```python
import torch

def _softmax(x, axis=-1):
    return torch.softmax(x, dim=axis)
def _gelu(x):
    return torch.nn.functional.gelu(x, approximate="tanh")
def _silu(x):
    return torch.nn.functional.silu(x)
def _sigmoid(x):
    return torch.sigmoid(x)
def _layernorm(x, axis=-1, eps=1e-5):
    m = x.mean(dim=axis, keepdim=True)
    v = x.var(dim=axis, keepdim=True, unbiased=False)
    return (x - m) / torch.sqrt(v + eps)
def _scatter_add(tgt, idx, src, axis=0):
    return tgt.clone().index_add_(axis, idx, src)
def _segment_sum(src, seg, n):
    out = torch.zeros((n,) + src.shape[1:], dtype=src.dtype)
    return out.index_add_(0, seg, src)
def _pad(x, pw):
    flat = tuple(v for pair in reversed(pw) for v in pair)
    return torch.nn.functional.pad(x, flat)

kernel_name = 'standalone__edge_featured_mpnn_gatedgcn__graph'

def setup_inputs():

    _w = (lambda a: (lambda t: t.to(torch.bfloat16) if t.is_floating_point() else t)(torch.as_tensor(a)))
    N, deg, D, De = 25000, 16, 128, max(0, 1)
    E = N * deg
    return {
        "x": _w((lambda *s: torch.randn(*s, dtype=torch.bfloat16) * 0.4)(N, D)),
        "e_attr": _w((lambda *s: torch.randn(*s, dtype=torch.bfloat16) * 0.8)(E, De)),
        "src": _w((lambda lo, hi, *s: torch.randint(lo, hi, s))(0, N, E).to(torch.int64)),
        "dst": _w((lambda lo, hi, *s: torch.randint(lo, hi, s))(0, N, E).to(torch.int64)),
        "w_a": _w((lambda *s: torch.randn(*s, dtype=torch.bfloat16) * 0.4)(D, D)),
        "w_b": _w((lambda *s: torch.randn(*s, dtype=torch.bfloat16) * 0.4)(D, D)),
        "w_c": _w((lambda *s: torch.randn(*s, dtype=torch.bfloat16) * 0.8)(De, D)),
        "w_u": _w((lambda *s: torch.randn(*s, dtype=torch.bfloat16) * 0.05)(D, D)),
        "w_v": _w((lambda *s: torch.randn(*s, dtype=torch.bfloat16) * 0.05)(D, D)),
    }

def reference(x, e_attr, src, dst, w_a, w_b, w_c, w_u, w_v):
    N, D = x.shape
    hv = x @ w_v
    g = x[dst] @ w_a + x[src] @ w_b + e_attr @ w_c
    eta = _sigmoid(g)
    denom = _segment_sum(eta, dst, N) + 1e-6
    eta_n = eta / denom[dst]
    agg = _segment_sum(eta_n * hv[src], dst, N)
    z = x @ w_u + agg
    relu_z = torch.where(z > 0, z, 0 * z)
    return x + relu_z
```

```cpp
#include <hip/hip_runtime.h>
#include <hip/hip_bf16.h>
#include <stdint.h>

namespace {

constexpr int NN = 25000;
constexpr int EE = 400000;
constexpr int RN = 391;
constexpr int NR = 64;
constexpr int NP = 128;
constexpr int CH = 3128;
constexpr int GT = 196;
constexpr int TROWS = 25096;
constexpr int NULLROW = 25088;
constexpr int CAP = 46;
constexpr int SLOTS = 392;

constexpr size_t TAB_BYTES = (size_t)4 * TROWS * 128;
constexpr size_t WS_BV = 0;
constexpr size_t WS_AU = WS_BV + TAB_BYTES;
constexpr size_t WS_WORD = WS_AU + TAB_BYTES;
constexpr size_t WS_DL = WS_WORD + (size_t)NP * CH * 4;
constexpr size_t WS_OFF = WS_DL + (size_t)NP * CH * 2;

constexpr float NLOG2E = -1.4426950408889634f;

typedef float f32x2 __attribute__((ext_vector_type(2)));
typedef float f32x4 __attribute__((ext_vector_type(4)));
typedef short s16x4 __attribute__((ext_vector_type(4)));
typedef __bf16 bf16x8 __attribute__((ext_vector_type(8)));
typedef __bf16 bf16x2 __attribute__((ext_vector_type(2)));
typedef uint32_t u32x2 __attribute__((ext_vector_type(2)));
typedef uint32_t u32x4 __attribute__((ext_vector_type(4)));
typedef int i32x4 __attribute__((ext_vector_type(4)));
#define LAS __attribute__((address_space(3)))
#define GAS __attribute__((address_space(1)))

__device__ __forceinline__ uint32_t pack_bf16(float lo, float hi) {
  f32x2 v = {lo, hi};
  return __builtin_bit_cast(uint32_t, __builtin_convertvector(v, bf16x2));
}
__device__ __forceinline__ void wave_fence() {
  __builtin_amdgcn_fence(__ATOMIC_ACQ_REL, "wavefront");
  __builtin_amdgcn_wave_barrier();
}
__device__ __forceinline__ void lds_barrier() {
  asm volatile("s_waitcnt lgkmcnt(0)\n\ts_barrier" ::: "memory");
}
__device__ __forceinline__ int wave_scan(int v) {
  v += __builtin_amdgcn_update_dpp(0, v, 0x111, 0xf, 0xf, false);
  v += __builtin_amdgcn_update_dpp(0, v, 0x112, 0xf, 0xf, false);
  v += __builtin_amdgcn_update_dpp(0, v, 0x114, 0xf, 0xf, false);
  v += __builtin_amdgcn_update_dpp(0, v, 0x118, 0xf, 0xf, false);
  v += __builtin_amdgcn_update_dpp(0, v, 0x142, 0xa, 0xf, false);
  v += __builtin_amdgcn_update_dpp(0, v, 0x143, 0xc, 0xf, false);
  return v;
}

constexpr int P_W = 0;
constexpr int P_X = 65536;
constexpr int P_ST = P_X + 208 * 256;
constexpr int P_CNT = P_ST + 8 * 2048;
constexpr int P_WORD = P_CNT + 256;
constexpr int P_DL = P_WORD + CH * 4;
constexpr int P_END = P_DL + CH * 2;

__global__ void __launch_bounds__(1024) k_prep(const uint16_t* __restrict__ x, const uint16_t* __restrict__ eattr,
                                               const int* __restrict__ src, const int* __restrict__ dst,
                                               const uint16_t* __restrict__ wa, const uint16_t* __restrict__ wb,
                                               const uint16_t* __restrict__ wu, const uint16_t* __restrict__ wv,
                                               char* __restrict__ ws) {
  __shared__ __attribute__((aligned(16))) char smem[P_END];
  const int tid = threadIdx.x, lane = tid & 63;
  const int wave = __builtin_amdgcn_readfirstlane(tid >> 6);
  const int b = blockIdx.x;
  const int tile = (b >> 4) * 8 + (b & 7), pair = (b >> 3) & 1;

  if (wave < 8) {
    const int node0 = tile * GT;
    {
      const uint16_t* w0 = pair ? wa : wb;
      const uint16_t* w1 = pair ? wu : wv;
      const uint16_t* wsel = (wave < 4) ? w0 : w1;
      const int rl = lane >> 4, cl = lane & 15;
#pragma unroll
      for (int i = 0; i < 8; ++i) {
        const int piece = (wave & 3) * 8 + i;
        const int row = piece * 4 + rl;
        const int g3 = (row & 3) | (((row >> 3) & 1) << 2);
        const int ch = cl ^ (g3 << 1);
        __builtin_amdgcn_global_load_lds((const GAS void*)(wsel + row * 128 + ch * 8),
                                         (LAS void*)(smem + P_W + (wave >> 2) * 32768 + piece * 1024), 16, 0, 0);
      }
#pragma unroll
      for (int i = 0; i < 7; ++i) {
        const int piece = i * 8 + wave;
        if (piece < 49) {
          const int row = piece * 4 + rl;
          const int ch = cl ^ (row & 15);
          int n = node0 + row;
          n = n < NN ? n : NN - 1;
          __builtin_amdgcn_global_load_lds((const GAS void*)(x + (size_t)n * 128 + ch * 8),
                                           (LAS void*)(smem + P_X + piece * 1024), 16, 0, 0);
        }
      }
    }
    if (pair == 0) {
      asm volatile("s_barrier" ::: "memory");
      asm volatile("s_barrier" ::: "memory");
    }
    asm volatile("s_waitcnt vmcnt(0)\n\ts_barrier" ::: "memory");

    const int g = wave & 3, half = wave >> 2;
    const int r = lane & 15, q = lane >> 4;
    bf16x8 wf[2][2][4];
    {
      const int qq = r >> 2, p = r & 3;
#pragma unroll
      for (int m = 0; m < 2; ++m)
#pragma unroll
        for (int T = 0; T < 2; ++T)
#pragma unroll
          for (int s = 0; s < 4; ++s) {
            s16x4 h[2];
#pragma unroll
            for (int hh = 0; hh < 2; ++hh) {
              const int row = 32 * s + 8 * q + 4 * hh + qq;
              const int g3 = (row & 3) | (((row >> 3) & 1) << 2);
              const int ch = (4 * g + 2 * T + (p >> 1)) ^ (g3 << 1);
              h[hh] = __builtin_amdgcn_ds_read_tr16_b64_v4i16(
                  (LAS s16x4*)(smem + P_W + m * 32768 + row * 256 + ch * 16 + (p & 1) * 8));
            }
            typedef short s16x8 __attribute__((ext_vector_type(8)));
            s16x8 f = __builtin_shufflevector(h[0], h[1], 0, 1, 2, 3, 4, 5, 6, 7);
            wf[m][T][s] = __builtin_bit_cast(bf16x8, f);
          }
    }
    char* tab = ws + (pair ? WS_AU : WS_BV) + (size_t)g * TROWS * 128;
    char* st = smem + P_ST + wave * 2048;
    const int nt = half ? 6 : 7;
    for (int t = 0; t < nt; ++t) {
      const int sub = half * 7 + t;
      bf16x8 xf[4];
#pragma unroll
      for (int s = 0; s < 4; ++s)
        xf[s] = *(const bf16x8*)(smem + P_X + (sub * 16 + r) * 256 + (((4 * s + q) ^ r) << 4));
      f32x4 acc[2][2];
#pragma unroll
      for (int m = 0; m < 2; ++m)
#pragma unroll
        for (int T = 0; T < 2; ++T) {
          f32x4 c = {0.f, 0.f, 0.f, 0.f};
#pragma unroll
          for (int s = 0; s < 4; ++s) c = __builtin_amdgcn_mfma_f32_16x16x32_bf16(wf[m][T][s], xf[s], c, 0, 0, 0);
          acc[m][T] = c;
        }
#pragma unroll
      for (int T = 0; T < 2; ++T) {
        const f32x4 a = acc[0][T] * NLOG2E, v = acc[1][T];
        u32x4 o = {pack_bf16(a[0], a[1]), pack_bf16(a[2], a[3]), pack_bf16(v[0], v[1]), pack_bf16(v[2], v[3])};
        *(u32x4*)(st + r * 128 + (((4 * T + q) ^ (r & 7)) << 4)) = o;
      }
      wave_fence();
#pragma unroll
      for (int i = 0; i < 2; ++i) {
        const int row = (lane >> 3) + 8 * i;
        const u32x4 o = *(const u32x4*)(st + row * 128 + (((lane & 7) ^ (row & 7)) << 4));
        const int rt = sub * 16 + row;
        if (rt < GT) *(u32x4*)(tab + (size_t)(node0 + rt) * 128 + (lane & 7) * 16) = o;
      }
      wave_fence();
    }
  } else if (pair == 0) {
    const int t = tid - 512, p = tile;
    const int e0 = p * CH;
    const int ne = (EE - e0) < CH ? (EE - e0) : CH;
    int* cnt = (int*)(smem + P_CNT);
    i32x4 d[2], s[2];
    u32x2 ea[2];
    bool ok[2];
#pragma unroll
    for (int k = 0; k < 2; ++k) {
      const int i = (k * 512 + t) * 4;
      ok[k] = i < ne;
      if (ok[k]) {
        d[k] = *(const i32x4*)(dst + e0 + i);
        s[k] = *(const i32x4*)(src + e0 + i);
        ea[k] = *(const u32x2*)(eattr + e0 + i);
      }
    }
    if (t < 64) cnt[t] = 0;
    if (p < 4 && t >= 64 && t < 72) {
      const u32x4 o = {0x71007100u, 0x71007100u, 0u, 0u};
      *(u32x4*)(ws + WS_BV + ((size_t)p * TROWS + NULLROW) * 128 + (t - 64) * 16) = o;
    }
    lds_barrier();
    int rg[2][4] = {{0, 0, 0, 0}, {0, 0, 0, 0}}, rk[2][4] = {{0, 0, 0, 0}, {0, 0, 0, 0}};
#pragma unroll
    for (int k = 0; k < 2; ++k)
      if (ok[k]) {
#pragma unroll
        for (int j = 0; j < 4; ++j) {
          rg[k][j] = (int)(((uint32_t)d[k][j] * 42909u) >> 24);
          rk[k][j] = atomicAdd(&cnt[rg[k][j]], 1);
        }
      }
    lds_barrier();
    const int c = cnt[lane];
    const int base = wave_scan(c) - c;
    if (t < 64) ((uint32_t*)(ws + WS_OFF))[t * NP + p] = (uint32_t)base | ((uint32_t)c << 16);
    uint32_t* sw = (uint32_t*)(smem + P_WORD);
    uint16_t* sd = (uint16_t*)(smem + P_DL);
#pragma unroll
    for (int k = 0; k < 2; ++k) {
#pragma unroll
      for (int j = 0; j < 4; ++j) {
        const int bs = __builtin_amdgcn_ds_bpermute(rg[k][j] << 2, base);
        if (ok[k]) {
          const int pos = bs + rk[k][j];
          const uint32_t ew = (j & 1) ? (ea[k][j >> 1] & 0xffff0000u) : (ea[k][j >> 1] << 16);
          sw[pos] = ew | (uint32_t)s[k][j];
          sd[pos] = (uint16_t)(d[k][j] - rg[k][j] * RN);
        }
      }
    }
    lds_barrier();
#pragma unroll
    for (int k = 0; k < 2; ++k) {
      const int i = (k * 512 + t) * 4;
      if (i < ne) *(u32x4*)(ws + WS_WORD + ((size_t)e0 + i) * 4) = *(const u32x4*)(sw + i);
    }
    if (t * 8 < ne) *(u32x4*)(ws + WS_DL + ((size_t)e0 + t * 8) * 2) = *(const u32x4*)(sd + t * 8);
  }
}

constexpr int E_LIST = 0;
constexpr int E_LIST_BYTES = 72192;
constexpr int E_ACC_BYTES = 2 * SLOTS * 32 * 4;
constexpr int E_CNT = E_ACC_BYTES;
constexpr int E_HIST = E_CNT + SLOTS * 4;
constexpr int E_ORD = E_HIST + 72 * 4;
constexpr int E_END = E_ORD + SLOTS * 4;
static_assert(E_LIST_BYTES >= (SLOTS * CAP + 8) * 4 && E_LIST_BYTES <= E_ACC_BYTES, "lds map");

constexpr float SH = 60.f;
constexpr float EPS1 = 8.673617379884035e-19f;
constexpr float EPSD = 1e-6f * 1152921504606846976.f;

__device__ __forceinline__ f32x4 unpack_add(s16x4 ident, uint32_t lo, uint32_t hi, f32x4 c) {
  u32x2 v = {lo, hi};
  return __builtin_amdgcn_mfma_f32_4x4x4bf16_1k(ident, __builtin_bit_cast(s16x4, v), c, 0, 0, 0);
}
__device__ __forceinline__ float exp2_sat(float t) {
  return __builtin_amdgcn_fmed3f(__builtin_amdgcn_exp2f(t), 0.f, 1.f);
}

struct Pair {
  u32x2 w;
  u32x4 g0, g1;
};

__device__ __forceinline__ void pair_load(Pair& P, u32x2 w, __amdgpu_buffer_rsrc_t rs, uint32_t coff) {
  P.w = w;
  P.g0 = __builtin_amdgcn_raw_buffer_load_b128(rs, (w[0] & 0xffffu) * 128u + coff, 0, 0);
  P.g1 = __builtin_amdgcn_raw_buffer_load_b128(rs, (w[1] & 0xffffu) * 128u + coff, 0, 0);
}
__device__ __forceinline__ void pair_compute(const Pair& P, s16x4 ident, f32x4 a, f32x4 cw, f32x4& den, f32x4& num) {
  const float e0 = __builtin_bit_cast(float, P.w[0] & 0xffff0000u);
  const float e1 = __builtin_bit_cast(float, P.w[1] & 0xffff0000u);
  const f32x4 z = {0.f, 0.f, 0.f, 0.f};
  const f32x4 t0 = unpack_add(ident, P.g0[0], P.g0[1], __builtin_elementwise_fma(cw, (f32x4)e0, a));
  const f32x4 t1 = unpack_add(ident, P.g1[0], P.g1[1], __builtin_elementwise_fma(cw, (f32x4)e1, a));
  const f32x4 v0 = unpack_add(ident, P.g0[2], P.g0[3], z);
  const f32x4 v1 = unpack_add(ident, P.g1[2], P.g1[3], z);
  f32x4 d0, d1, r;
#pragma unroll
  for (int i = 0; i < 4; ++i) {
    d0[i] = exp2_sat(t0[i]);
    d1[i] = exp2_sat(t1[i]);
  }
  d0 += EPS1;
  d1 += EPS1;
  const f32x4 p = d0 * d1;
#pragma unroll
  for (int i = 0; i < 4; ++i) r[i] = __builtin_amdgcn_rcpf(p[i]);
  den = __builtin_elementwise_fma(d0 + d1, r, den);
  num = __builtin_elementwise_fma(__builtin_elementwise_fma(v0, d1, v1 * d0), r, num);
}

__global__ void __launch_bounds__(1024) k_edge(const uint16_t* __restrict__ x, const uint16_t* __restrict__ wc,
                                               const char* __restrict__ ws, uint16_t* __restrict__ out) {
  __shared__ __attribute__((aligned(16))) char smem[E_END];
  const int tid = threadIdx.x, lane = tid & 63;
  const int wave = __builtin_amdgcn_readfirstlane(tid >> 6);
  const int b = blockIdx.x;
  const int g = b & 3, range = ((b >> 2) & 1) * 32 + (b >> 3);
  const int node0 = range * RN;
  const int nvalid = (NN - node0) < RN ? (NN - node0) : RN;

  uint32_t* lists = (uint32_t*)(smem + E_LIST);
  int* cnt = (int*)(smem + E_CNT);
  int* hist = (int*)(smem + E_HIST);
  uint32_t* order = (uint32_t*)(smem + E_ORD);
  const uint32_t* words = (const uint32_t*)(ws + WS_WORD);
  const uint16_t* dls = (const uint16_t*)(ws + WS_DL);

  const uint32_t* offp = (const uint32_t*)(ws + WS_OFF) + range * NP + wave * 8;
  uint32_t off[8];
#pragma unroll
  for (int j = 0; j < 8; ++j) off[j] = offp[j];

  {
    const u32x4 nul = {NULLROW, NULLROW, NULLROW, NULLROW};
    for (int i = tid; i < E_LIST_BYTES / 16; i += 1024) *(u32x4*)(smem + i * 16) = nul;
    if (tid < SLOTS + 72) cnt[tid] = (tid == SLOTS + 65) ? 16 : 0;
  }
  uint32_t w[8], d[8];
#pragma unroll
  for (int j = 0; j < 8; ++j) {
    const int idx = (wave * 8 + j) * CH + (int)(off[j] & 0xffffu) + lane;
    w[j] = 0;
    d[j] = 0;
    if (lane < (int)(off[j] >> 16)) {
      w[j] = words[idx];
      d[j] = dls[idx];
    }
  }
  const int c = lane & 7, slot = lane >> 3;
  const u32x2 wcv = *(const u32x2*)(wc + g * 32 + c * 4);
  lds_barrier();
#pragma unroll
  for (int j = 0; j < 8; ++j)
    if (lane < (int)(off[j] >> 16)) {
      const int rank = atomicAdd(&cnt[d[j]], 1);
      if (rank < CAP) lists[d[j] * CAP + rank] = w[j];
    }
#pragma unroll
  for (int j = 0; j < 8; ++j) {
    const int n = (int)(off[j] >> 16);
    for (int i = 64 + lane; i < n; i += 64) {
      const int idx = (wave * 8 + j) * CH + (int)(off[j] & 0xffffu) + i;
      const uint32_t ww = words[idx], dd = dls[idx];
      const int rank = atomicAdd(&cnt[dd], 1);
      if (rank < CAP) lists[dd * CAP + rank] = ww;
    }
  }
  lds_barrier();
  int deg = 0, key = 0, rk = 0;
  if (tid < SLOTS) {
    deg = cnt[tid];
    if (deg > CAP) hist[64] = 1;
    key = 63 - (deg < 63 ? deg : 63);
    rk = atomicAdd(&hist[key], 1);
  }
  lds_barrier();

  s16x4 ident;
#pragma unroll
  for (int k = 0; k < 4; ++k) ident[k] = ((lane & 3) == k) ? (short)0x3f80 : (short)0;
  const f32x4 zero4 = {0.f, 0.f, 0.f, 0.f};
  const f32x4 cw = unpack_add(ident, wcv[0], wcv[1], zero4) * NLOG2E;
  const char* bv = ws + WS_BV + (size_t)g * TROWS * 128;
  const char* au = ws + WS_AU + ((size_t)g * TROWS + node0) * 128;
  const uint32_t coff = c * 16;

  if (__builtin_amdgcn_readfirstlane(hist[64]) != 0) {
    float* den = (float*)smem;
    float* num = den + SLOTS * 32;
    for (int i = tid; i < E_ACC_BYTES / 16; i += 1024) *(f32x4*)(smem + i * 16) = zero4;
    lds_barrier();
    for (int j = 0; j < 8; ++j) {
      const uint32_t o = offp[j];
      const int n = (int)(o >> 16);
      const int base = (wave * 8 + j) * CH + (int)(o & 0xffffu);
      for (int i0 = 0; i0 < n; i0 += 8) {
        const bool act = i0 + slot < n;
        const int idx = base + (act ? i0 + slot : 0);
        const uint32_t ww = words[idx], dd = dls[idx];
        const u32x4 gv = *(const u32x4*)(bv + (ww & 0xffffu) * 128u + coff);
        const u32x2 av = *(const u32x2*)(au + dd * 128u + coff);
        const float e = __builtin_bit_cast(float, ww & 0xffff0000u);
        const f32x4 t = unpack_add(ident, gv[0], gv[1], unpack_add(ident, av[0], av[1], cw * e));
        const f32x4 v = unpack_add(ident, gv[2], gv[3], zero4);
        if (act) {
#pragma unroll
          for (int k = 0; k < 4; ++k) {
            const float s = __builtin_amdgcn_rcpf(1.f + __builtin_amdgcn_exp2f(t[k]));
            atomicAdd(&den[dd * 32 + c * 4 + k], s);
            atomicAdd(&num[dd * 32 + c * 4 + k], s * v[k]);
          }
        }
      }
    }
    lds_barrier();
    for (int o = wave; o < SLOTS / 8; o += 16) {
      const int node = o * 8 + slot;
      const int nc = node < nvalid ? node : 0;
      const u32x4 av = *(const u32x4*)(au + nc * 128u + coff);
      const u32x2 xv = *(const u32x2*)(x + (size_t)(node0 + nc) * 128 + g * 32 + c * 4);
      const f32x4 dn = *(const f32x4*)(den + node * 32 + c * 4), nm = *(const f32x4*)(num + node * 32 + c * 4);
      f32x4 z = unpack_add(ident, av[2], av[3], zero4);
#pragma unroll
      for (int k = 0; k < 4; ++k) z[k] = fmaxf(fmaf(nm[k], __builtin_amdgcn_rcpf(dn[k] + 1e-6f), z[k]), 0.f);
      z = unpack_add(ident, xv[0], xv[1], z);
      const u32x2 ov = {pack_bf16(z[0], z[1]), pack_bf16(z[2], z[3])};
      if (node < nvalid) *(u32x2*)(out + (size_t)(node0 + node) * 128 + g * 32 + c * 4) = ov;
    }
    return;
  }

  if (wave < 7) {
    const int h = hist[lane];
    const int base = wave_scan(h) - h;
    const int bs = __builtin_amdgcn_ds_bpermute(key << 2, base);
    if (tid < SLOTS) order[bs + rk] = (uint32_t)tid | ((uint32_t)deg << 16);
  }
  lds_barrier();

  const __amdgpu_buffer_rsrc_t rs = __builtin_amdgcn_make_buffer_rsrc((void*)bv, 0, TROWS * 128, 0x00020000);
  const f32x4 sh4 = {-SH, -SH, -SH, -SH};
  int oct = wave;
  while (oct < SLOTS / 8) {
    const uint32_t od = order[oct * 8 + slot];
    int nxt = 0;
    if (lane == 0) nxt = atomicAdd(&hist[65], 1);
    const int node = od & 0xffffu;
    const int np = (__builtin_amdgcn_readfirstlane(od >> 16) + 1) >> 1;
    const int nc = node < nvalid ? node : 0;
    const u32x2* lp = (const u32x2*)(lists + node * CAP);
    const u32x4 av = *(const u32x4*)(au + nc * 128u + coff);
    const u32x2 xv = *(const u32x2*)(x + (size_t)(node0 + nc) * 128 + g * 32 + c * 4);
    f32x4 den = {EPSD, EPSD, EPSD, EPSD}, num = zero4;
    Pair A, B;
    pair_load(A, lp[0], rs, coff);
    u32x2 wn = lp[1];
    const f32x4 a = unpack_add(ident, av[0], av[1], sh4);
    int i = 1;
    for (;;) {
      if (i >= np) {
        pair_compute(A, ident, a, cw, den, num);
        break;
      }
      pair_load(B, wn, rs, coff);
      wn = lp[i + 1];
      pair_compute(A, ident, a, cw, den, num);
      ++i;
      if (i >= np) {
        pair_compute(B, ident, a, cw, den, num);
        break;
      }
      pair_load(A, wn, rs, coff);
      wn = lp[i + 1];
      pair_compute(B, ident, a, cw, den, num);
      ++i;
    }
    f32x4 z = unpack_add(ident, av[2], av[3], zero4);
#pragma unroll
    for (int k = 0; k < 4; ++k) z[k] = fmaxf(fmaf(num[k], __builtin_amdgcn_rcpf(den[k]), z[k]), 0.f);
    z = unpack_add(ident, xv[0], xv[1], z);
    const u32x2 ov = {pack_bf16(z[0], z[1]), pack_bf16(z[2], z[3])};
    if (node < nvalid) *(u32x2*)(out + (size_t)(node0 + node) * 128 + g * 32 + c * 4) = ov;
    oct = __builtin_amdgcn_readfirstlane(nxt);
  }
}

}

extern "C" void kernel_launch(void* const* d_in, const int* in_sizes, int n_in, void* d_out, int out_size, void* d_ws,
                              size_t ws_size, hipStream_t stream) {
  const uint16_t* x = (const uint16_t*)d_in[0];
  const uint16_t* ea = (const uint16_t*)d_in[1];
  const int* src = (const int*)d_in[2];
  const int* dst = (const int*)d_in[3];
  const uint16_t* wa = (const uint16_t*)d_in[4];
  const uint16_t* wb = (const uint16_t*)d_in[5];
  const uint16_t* wc = (const uint16_t*)d_in[6];
  const uint16_t* wu = (const uint16_t*)d_in[7];
  const uint16_t* wv = (const uint16_t*)d_in[8];
  k_prep<<<256, 1024, 0, stream>>>(x, ea, src, dst, wa, wb, wu, wv, (char*)d_ws);
  k_edge<<<256, 1024, 0, stream>>>(x, wc, (const char*)d_ws, (uint16_t*)d_out);
}
```

```cpp
#include <hip/hip_runtime.h>
#include <hip/hip_bf16.h>
#include <stdint.h>

#define STAMP(i)
#define STAMP_FLUSH(lo, hi)
#define ASM_TAIL ""

#define BV_ROW 128
#define X_ROW 256
#define XAHEAD 1
#define XROT 0
#define POLL_A 4
#define POLL_B 4
#define MAP 0
#define PREFETCH 0
#define BV_PRIO 1
#define BV_GATE 256
#define BV_PRIO2 3
#define SRC_ORDER 1
#define X1 12
#define X3 15
#define HOLD 0
#define DLY_P 25
#define DLY_N 90
#define WORDS_AT 1
#define T24 0
#define CODE_PF 1
#define SORT_PRIO 2
#define TAB_RECORDS TROWS

namespace {

constexpr int NN = 25000;
constexpr int RN = 391;
constexpr int NP = 160;
constexpr int CH = 2500;
constexpr int CHP = 2696;
constexpr int SEG = NP / 8;
constexpr int NULL0 = 64 * RN;
constexpr int TROWS = NULL0 + 64;
constexpr int SLOTS = 392;
constexpr int OFFS = 640;
constexpr unsigned SPIN_MAX = 1u << 20;

constexpr size_t WS_EPOCH = 0;
constexpr size_t WS_FLAG = 4096;
constexpr size_t WS_OFF = WS_FLAG + 256 * 128;
constexpr size_t WS_WORD = WS_OFF + (size_t)NP * OFFS;
constexpr size_t WS_DL = WS_WORD + (size_t)NP * CHP * 4;
constexpr size_t WS_BV = (WS_DL + (size_t)NP * CHP * 2 + 4095) / 4096 * 4096;

constexpr int L_CNT = 0;
constexpr int L_HIST = 6400;
constexpr int L_PCNT = L_HIST + 256;
constexpr int L_SYNC = L_PCNT + 256;
constexpr int L_ZEND = L_SYNC + 64;
constexpr int L_WC = L_ZEND;
constexpr int L_TURN = L_WC + 64;
constexpr int L_ORD = L_TURN + 256;
constexpr int L_X = 8960;
constexpr int L_WAU = L_X + 400 * 256;
constexpr int L_LIST = L_WAU + 16384;
constexpr int L_WBV = L_LIST;
constexpr int L_SW = L_WBV + 16384;
constexpr int L_SD = L_SW + CHP * 4;
constexpr int L_END = 163840;
constexpr int L_JUNK = L_END - 2048;
constexpr int LIST_STEPS = (L_JUNK - L_LIST) / 64;
static_assert(L_ORD + SLOTS * 4 <= L_X && L_SD + CHP * 2 <= L_END && (L_END - L_LIST) >= 131 * 256, "lds map");
enum { Y_SORT = 0, Y_WBV, Y_WAU, Y_WFRAG, Y_PUB, Y_BV, Y_AU, Y_TAG, Y_SLOW, Y_ORD, Y_ENT, Y_STG, Y_TAB };

constexpr float NLOG2E = -1.4426950408889634f;
constexpr int SC1 = 16;

typedef float f32x2 __attribute__((ext_vector_type(2)));
typedef float f32x4 __attribute__((ext_vector_type(4)));
typedef short s16x4 __attribute__((ext_vector_type(4)));
typedef short s16x8 __attribute__((ext_vector_type(8)));
typedef __bf16 bf16x8 __attribute__((ext_vector_type(8)));
typedef __bf16 bf16x2 __attribute__((ext_vector_type(2)));
typedef uint32_t u32x2 __attribute__((ext_vector_type(2)));
typedef uint32_t u32x4 __attribute__((ext_vector_type(4)));
typedef uint32_t u32x8 __attribute__((ext_vector_type(8)));
typedef uint32_t u32x16 __attribute__((ext_vector_type(16)));
typedef __amdgpu_buffer_rsrc_t rsrc_t;
#define LAS __attribute__((address_space(3)))
#define SEG_SGPRS "s64", "s65", "s66", "s67", "s68", "s69", "s70", "s71", "s72", "s73", "s74", "s75"

__device__ __forceinline__ uint32_t pack_bf16(float lo, float hi) {
  f32x2 v = {lo, hi};
  return __builtin_bit_cast(uint32_t, __builtin_convertvector(v, bf16x2));
}
__device__ __forceinline__ rsrc_t make_rsrc(const void* p, uint32_t bytes) {
  return __builtin_amdgcn_make_buffer_rsrc((void*)p, 0, bytes, 0x00020000);
}
__device__ __forceinline__ void wave_fence() {
  __builtin_amdgcn_fence(__ATOMIC_ACQ_REL, "wavefront");
  __builtin_amdgcn_wave_barrier();
}
__device__ __forceinline__ void lds_barrier() { asm volatile("s_waitcnt lgkmcnt(0)\n\ts_barrier" ::: "memory"); }
__device__ __forceinline__ void lds_arrive(uint32_t ctr) {
  asm volatile("s_mov_b64 exec, 1\n\tds_add_u32 %0, %1\n\ts_mov_b64 exec, -1" ::"v"(ctr), "v"(1) : "memory");
}
__device__ __forceinline__ void lds_wait(uint32_t ctr, int target) {
  int t;
  asm volatile("Lw%=:\n\tds_read_b32 %0, %1\n\ts_waitcnt lgkmcnt(0)\n\tv_cmp_gt_i32 vcc, %2, %0\n\ts_cbranch_vccnz Lw%="
               : "=&v"(t) : "v"(ctr), "s"(target) : "memory", "vcc");
}
__device__ __forceinline__ void lds_wait_idle(uint32_t ctr, int target) {
  int t;
  asm volatile("s_branch Lw%=\n\tLs%=:\n\ts_sleep 2\n\tLw%=:\n\tds_read_b32 %0, %1\n\ts_waitcnt lgkmcnt(0)\n\t"
               "v_cmp_gt_i32 vcc, %2, %0\n\ts_cbranch_vccnz Ls%="
               : "=&v"(t) : "v"(ctr), "s"(target) : "memory", "vcc");
}
__device__ __forceinline__ bool lds_arrive_last(uint32_t ctr, int n) {
  int t;
  asm volatile("s_mov_b64 exec, 1\n\tds_add_rtn_u32 %0, %1, %2\n\ts_mov_b64 exec, -1\n\ts_waitcnt lgkmcnt(0)"
               : "=&v"(t) : "v"(ctr), "v"(1) : "memory");
  return __builtin_amdgcn_readfirstlane(t) == n - 1;
}
__device__ __forceinline__ int wave_scan(int v) {
  v += __builtin_amdgcn_update_dpp(0, v, 0x111, 0xf, 0xf, false);
  v += __builtin_amdgcn_update_dpp(0, v, 0x112, 0xf, 0xf, false);
  v += __builtin_amdgcn_update_dpp(0, v, 0x114, 0xf, 0xf, false);
  v += __builtin_amdgcn_update_dpp(0, v, 0x118, 0xf, 0xf, false);
  v += __builtin_amdgcn_update_dpp(0, v, 0x142, 0xa, 0xf, false);
  v += __builtin_amdgcn_update_dpp(0, v, 0x143, 0xc, 0xf, false);
  return v;
}
__device__ __forceinline__ int wave_scan_max(int v) {
  v = max(v, __builtin_amdgcn_update_dpp(0, v, 0x111, 0xf, 0xf, false));
  v = max(v, __builtin_amdgcn_update_dpp(0, v, 0x112, 0xf, 0xf, false));
  v = max(v, __builtin_amdgcn_update_dpp(0, v, 0x114, 0xf, 0xf, false));
  v = max(v, __builtin_amdgcn_update_dpp(0, v, 0x118, 0xf, 0xf, false));
  v = max(v, __builtin_amdgcn_update_dpp(0, v, 0x142, 0xa, 0xf, false));
  v = max(v, __builtin_amdgcn_update_dpp(0, v, 0x143, 0xc, 0xf, false));
  return v;
}
__device__ __forceinline__ void dma4k(u32x4 srd, uint32_t soff, uint32_t v0, uint32_t v1, uint32_t v2, uint32_t v3,
                                      uint32_t lds_dst) {
  uint32_t keep;
  asm volatile("s_mov_b32 %0, m0\n\ts_mov_b32 m0, %7\n\ts_nop 3\n\t"
               "buffer_load_dwordx4 %1, %5, %6 offen lds\n\t"
               "buffer_load_dwordx4 %2, %5, %6 offen offset:1024 lds\n\t"
               "buffer_load_dwordx4 %3, %5, %6 offen offset:2048 lds\n\t"
               "buffer_load_dwordx4 %4, %5, %6 offen offset:3072 lds\n\t"
               "s_mov_b32 m0, %0"
               : "=&s"(keep) : "v"(v0), "v"(v1), "v"(v2), "v"(v3), "s"(srd), "s"(soff), "s"(lds_dst) : "memory");
}
__device__ __forceinline__ u32x4 make_srd(const void* p, uint32_t records, uint32_t stride = 0) {
  const uint64_t a = (uint64_t)p;
  return u32x4{(uint32_t)a, (uint32_t)(a >> 32) | (stride << 16), records, 0x00020000u};
}
template <int SLEEP>
__device__ __forceinline__ uint32_t poll_tagged(const char* p, bool active, uint32_t tlo, uint32_t thi) {
  uint32_t lo = 0;
  bool done = false, wait = active;
#pragma unroll 1
  for (unsigned spin = 0; spin < SPIN_MAX; ++spin) {
    if (wait) {
      const uint64_t x = __hip_atomic_load((const uint64_t*)p, __ATOMIC_RELAXED, __HIP_MEMORY_SCOPE_AGENT);
      lo = (uint32_t)x;
      wait = (uint32_t)(x >> 32) != thi || (lo >> 24) != tlo;
    }
    done = __builtin_amdgcn_ballot_w64(wait) == 0;
    if (done) break;
    __builtin_amdgcn_s_sleep(SLEEP);
  }
  __builtin_amdgcn_fence(__ATOMIC_ACQUIRE, "wavefront");
  asm volatile("" ::: "memory");
  return active && done ? (lo & 0xffffffu) : 0u;
}
__device__ __forceinline__ void store_tagged(char* p, uint32_t payload, uint32_t tlo, uint32_t thi) {
  __hip_atomic_store((uint64_t*)p, ((uint64_t)thi << 32) | (tlo << 24) | payload, __ATOMIC_RELAXED, __HIP_MEMORY_SCOPE_AGENT);
}
__device__ __forceinline__ f32x4 unpack_add(s16x4 ident, uint32_t lo, uint32_t hi, f32x4 c) {
  u32x2 v = {lo, hi};
  return __builtin_amdgcn_mfma_f32_4x4x4bf16_1k(ident, __builtin_bit_cast(s16x4, v), c, 0, 0, 0);
}
template <typename T>
__device__ __forceinline__ LAS T* lds_at(uint32_t a) { return (LAS T*)(uintptr_t)a; }
__device__ __forceinline__ uint32_t opaque(uint32_t a) {
  asm volatile("" : "+v"(a));
  return a;
}
__device__ __forceinline__ uint32_t uni(uint32_t a) { return __builtin_amdgcn_readfirstlane(opaque(a)); }
__device__ __forceinline__ int oct_of(int j) {
  const int w = j >> 2, i = j & 3;
  const int o = (w ^ (i == 0 ? (w < 4 ? 2 : 0) : i == 1 ? X1 : i == 2 ? 0 : X3)) + 16 * i;
  return o > 48 ? 63 : o;
}
__device__ __forceinline__ int turn_of(int o) {
  const int x = o < 4 ? 2 : o < 16 ? 0 : o < 32 ? X1 : o < 48 ? 0 : X3;
  return 4 * ((o & 15) ^ x) + (o >> 4);
}

__global__ void __launch_bounds__(1024) k_fused(const uint16_t* __restrict__ x, const uint16_t* __restrict__ eattr,
                                                const int* __restrict__ src, const int* __restrict__ dst,
                                                const uint16_t* __restrict__ wa, const uint16_t* __restrict__ wb,
                                                const uint16_t* __restrict__ wc, const uint16_t* __restrict__ wu,
                                                const uint16_t* __restrict__ wv, char* ws, uint16_t* __restrict__ out) {
  __shared__ __attribute__((aligned(16))) char smem[L_END];
  STAMP(0);
  uint64_t pc0;
  asm volatile("s_getpc_b64 %0\nLfused_first:" : "=s"(pc0));
  asm volatile("" ::"s"(x), "s"(eattr), "s"(src), "s"(dst), "s"(wa), "s"(wb), "s"(wc), "s"(wu), "s"(wv), "s"(ws), "s"(out));
  const int tid = threadIdx.x, lane = tid & 63;
  const int wave = __builtin_amdgcn_readfirstlane(tid >> 6);
  const int b = blockIdx.x;
  const int g = b & 3, range = ((b >> 2) & 1) * 32 + (b >> 3);
  const int node0 = range * RN;
  const int nvalid = (NN - node0) < RN ? (NN - node0) : RN;
  const int fh = 8 - ((g >> 1) << 3);
  const uint32_t lds0 = (uint32_t)(uintptr_t)(LAS char*)smem;
  int* cnt = (int*)(smem + L_CNT);
  int* sync = (int*)(smem + L_SYNC);
  const uint32_t ysync = lds0 + L_SYNC;
  const rsrc_t r_word = make_rsrc(ws + WS_WORD, NP * CHP * 4);
  const rsrc_t r_dl = make_rsrc(ws + WS_DL, NP * CHP * 2);
  char* const tab = ws + WS_BV + (size_t)g * TROWS * 128;
  const f32x4 zero4 = {0.f, 0.f, 0.f, 0.f};
  uint32_t tlo = 0, thi = 0;
  bool poller = false;
#define LOAD_EPOCH() __hip_atomic_load((const uint64_t*)(ws + WS_EPOCH), __ATOMIC_RELAXED, __HIP_MEMORY_SCOPE_AGENT)
#define SET_TAG(ep)                                      \
  do {                                                   \
    const uint64_t t_ = (ep) + 0x9E3779B97F000000ull;    \
    tlo = (uint32_t)t_ >> 24;                            \
    thi = (uint32_t)(t_ >> 32);                          \
  } while (0)

  int ln, c, slot, slow_path;
  s16x4 ident;
  f32x4 cw;
  uint32_t k_au, k_x, lb, off[4];
  u32x4 au[4], tq;
  u32x2 xv[4];
  auto setup = [&]() {
    ln = opaque(lane), c = ln & 7, slot = ln >> 3;
    uint32_t rec[4];
#pragma unroll
    for (int i = 0; i < 4; ++i) {
      const int o = oct_of(wave * 4 + i);
      rec[i] = *(const uint32_t*)(smem + L_ORD + ((o < 49 ? o : 0) * 8 + slot) * 4);
    }
    tq = *lds_at<u32x4>(opaque(lds0 + L_TURN + wave * 16));
    slow_path = sync[Y_SLOW];
    const u32x2 wv2 = *(const u32x2*)(smem + L_WC + c * 8);
    const uint32_t one = 0x3f80u << ((ln & 1) << 4);
    ident = __builtin_bit_cast(s16x4, u32x2{(ln & 2) ? 0u : one, (ln & 2) ? one : 0u});
    k_au = (fh + c) << 4, k_x = g * 64 + c * 8;
#pragma unroll
    for (int i = 0; i < 4; ++i) {
      au[i] = *(const u32x4*)(smem + L_X + (rec[i] ^ k_au));
      xv[i] = *(const u32x2*)(smem + L_X + (rec[i] ^ k_x));
      off[i] = opaque((rec[i] & 0xffffff00u) | (c * 8));
    }
    cw = unpack_add(ident, wv2[0], wv2[1], zero4) * NLOG2E;
    lb = opaque(lds0 + L_LIST + (tq[0] >> 8) * 64 + slot * 4);
    asm volatile("" : "+v"(cw));
  };

  if (wave >= 8) {
    int w = wave - 8;
    asm volatile("s_setprio %1\n\ts_barrier" : "+s"(w) : "i"(BV_PRIO) : "memory");
    const int r = lane & 15, q = lane >> 4;
    int w0 = w + (MAP == 0 ? g : MAP == 1 ? (g & 2) : 0) * XROT;
    auto rot = [&](int k) { const int t = w0 + 8 * k; return k == 3 ? 24 : t < 24 ? t : t - 24; };
    const u32x4 srd_x = make_srd(x + (size_t)node0 * 128, nvalid * X_ROW);
    auto load_tile = [&](int t) {
      const int l = opaque(lane);
      const uint32_t xo = (l >> 4) * 256 + (((l & 15) ^ (l >> 4)) << 4);
      dma4k(srd_x, t * 4096, xo, xo ^ 64, xo ^ 128, xo ^ 192, lds0 + L_X + t * 4096);
    };
    {
      const uint16_t* wm = r < 8 ? (w < 4 ? wb : wa) : (w < 4 ? wv : wu);
      u32x16 y;
      asm volatile(
          "global_load_dwordx2 v[96:97], %[a], off offset:0\n\t"
          "global_load_dwordx2 v[98:99], %[a], off offset:256\n\t"
          "global_load_dwordx2 v[100:101], %[a], off offset:512\n\t"
          "global_load_dwordx2 v[102:103], %[a], off offset:768\n\t"
          "global_load_dwordx2 v[104:105], %[a], off offset:1024\n\t"
          "global_load_dwordx2 v[106:107], %[a], off offset:1280\n\t"
          "global_load_dwordx2 v[108:109], %[a], off offset:1536\n\t"
          "global_load_dwordx2 v[110:111], %[a], off offset:1792\n\t"
          : "=&{v[96:111]}"(y) : [a] "v"(wm + ((w & 3) * 32 + q * 8) * 128 + g * 32 + (r & 7) * 4) : "memory");
      if (HOLD == 0 || b >= NP) {
        load_tile(rot(0));
        asm volatile("s_waitcnt vmcnt(4)" ::: "memory");
      } else {
        asm volatile("s_waitcnt vmcnt(0)" ::: "memory");
      }
      asm volatile("" : "+{v[96:111]}"(y)::"memory");
      const uint32_t a = lds0 + (w < 4 ? L_WBV : L_WAU) + (w & 3) * 4096 + (q * 16 + (r & 7) * 2 + (r >> 3)) * 16;
#pragma unroll
      for (int i = 0; i < 4; ++i) {
        u32x4 f;
#pragma unroll
        for (int j = 0; j < 4; ++j)
          f[j] = __builtin_amdgcn_perm(y[4 * j + 2 + (i >> 1)], y[4 * j + (i >> 1)], (i & 1) ? 0x07060302u : 0x05040100u);
        *lds_at<u32x4>(a + i * 1024) = f;
      }
      lds_arrive(ysync + 4 * Y_WBV);
      if (w == 6) {
        uint32_t keep, len;
        asm volatile("s_mov_b32 %0, Lfused_last-Lfused_first" : "=s"(len));
        asm volatile("s_mov_b32 %0, m0\n\ts_mov_b32 m0, %4\n\ts_nop 0\n\tbuffer_load_dword %1, %2, %3 offen lds\n\ts_mov_b32 m0, %0"
                     : "=&s"(keep) : "v"(lane * 128), "s"(make_srd((const void*)pc0, len)), "s"(((b >> 3) & 1) * 8192),
                       "s"(lds0 + L_JUNK) : "memory");
      }
      if (HOLD != 0 && b < NP) {
        lds_wait_idle(ysync + 4 * Y_STG, HOLD == 1 ? 8 : HOLD == 2 ? 9 : 12);
        load_tile(rot(0));
      }
      if (w == T24) load_tile(24);
      if (XAHEAD >= 2) load_tile(rot(1));
      if (XAHEAD >= 3) load_tile(rot(2));
    }
    bf16x8 wf[4][4];
    auto load_w = [&](int img) {
      const uint32_t a = opaque(lds0 + img + lane * 16);
#pragma unroll
      for (int s = 0; s < 4; ++s)
#pragma unroll
        for (int i = 0; i < 4; ++i) wf[i][s] = *lds_at<bf16x8>(a + s * 4096 + i * 1024);
    };
    const float scale = (r & 1) ? 1.f : NLOG2E;
    const uint32_t a_frag = lds0 + L_X + r * 256 + ((q ^ r) << 4);
    bf16x8 xf[4];
    auto read_x = [&](int t) {
      const uint32_t a = opaque(a_frag + t * 4096);
#pragma unroll
      for (int s = 0; s < 4; ++s) xf[s] = *lds_at<bf16x8>(a ^ (s << 6));
      __builtin_amdgcn_sched_barrier(0);
    };
    auto product = [&](u32x2 (&o)[4], int next) {
      f32x4 acc[4] = {zero4, zero4, zero4, zero4};
      const uint32_t a = next ? opaque(lds0 + next + lane * 16) : 0u;
#pragma unroll
      for (int s = 0; s < 4; ++s) {
#pragma unroll
        for (int i = 0; i < 4; ++i) acc[i] = __builtin_amdgcn_mfma_f32_16x16x32_bf16(xf[s], wf[i][s], acc[i], 0, 0, 0);
        if (next) {
          __builtin_amdgcn_sched_barrier(0);
#pragma unroll
          for (int i = 0; i < 4; ++i) wf[i][s] = *lds_at<bf16x8>(a + s * 4096 + i * 1024);
          __builtin_amdgcn_sched_barrier(0);
        }
      }
#pragma unroll
      for (int i = 0; i < 4; ++i) acc[i] *= scale;
#pragma unroll
      for (int m = 0; m < 4; ++m) o[m] = u32x2{pack_bf16(acc[0][m], acc[1][m]), pack_bf16(acc[2][m], acc[3][m])};
    };
    const rsrc_t r_tab = make_rsrc(tab + (size_t)node0 * 128, nvalid * BV_ROW);
    auto store_bv = [&](int t, const u32x2 (&o)[4]) {
      const int l = opaque(lane);
#pragma unroll
      for (int m = 0; m < 4; ++m)
        __builtin_amdgcn_raw_buffer_store_b64(o[m], r_tab, (l >> 4) * 512 + (l & 15) * 8 + m * 128, t * 2048, SC1);
    };
    auto put_au = [&](int t, int next) {
      u32x2 y[4];
      product(y, next);
      wave_fence();
      const int l = opaque(lane), r = l & 15, q = l >> 4;
      const uint32_t a_put = lds0 + L_X + q * 1024 + (((fh + (r >> 1)) ^ (4 * q)) << 4) + (r & 1) * 8 + t * 4096;
#pragma unroll
      for (int m = 0; m < 4; ++m) *lds_at<u32x2>((a_put ^ (m << 4)) + m * 256) = y[m];
      __builtin_amdgcn_sched_barrier(0);
    };
    auto hold_bv = [&](u32x2 (&o)[4], int next) {
      product(o, next);
#pragma unroll
      for (int m = 0; m < 4; ++m) asm volatile("" : "+v"(o[m]));
      __builtin_amdgcn_sched_barrier(0);
    };
    lds_wait_idle(ysync + 4 * Y_WBV, 8);
    load_w(L_WBV);
    STAMP(1);
    u32x2 o[3][4] = {};
    asm volatile("s_waitcnt vmcnt(%0)" ::"i"(4 * XAHEAD - 4) : "memory");
    if (XAHEAD < 3) load_tile(rot(XAHEAD));
    if (w == T24) {
      read_x(24);
      hold_bv(o[1], 0);
    }
    read_x(rot(0));
    hold_bv(o[0], L_WAU);
    put_au(rot(0), 0);
    if (w == T24) {
      read_x(24);
      put_au(24, 0);
    }
    __builtin_amdgcn_s_setprio(BV_PRIO2);
    asm volatile("s_waitcnt vmcnt(%0)" ::"i"(XAHEAD >= 2 ? 4 : 0) : "memory");
    read_x(rot(1));
    if (XAHEAD == 1) load_tile(rot(2));
    if (w == T24) store_bv(24, o[1]);
    put_au(rot(1), L_WBV);
    lds_arrive(ysync + 4 * Y_WFRAG);
    hold_bv(o[1], 0);
    asm volatile("s_waitcnt vmcnt(0)" ::: "memory");
    read_x(rot(2));
    hold_bv(o[2], L_WAU);
    STAMP(6);
    put_au(rot(2), 0);
    if (w == 7 && lane < 8) {
      const u32x4 y = {0x71007100u, 0x71007100u, 0u, 0u};
      __builtin_amdgcn_raw_buffer_store_b128(y, make_rsrc(tab + (size_t)(NULL0 + range) * 128, 128), lane * 16, 0, SC1);
    }
    lds_arrive(ysync + 4 * Y_AU);
    STAMP(5);
    const uint64_t ep = LOAD_EPOCH();
    lds_wait_idle(ysync + 4 * Y_TAG, 8);
    lds_wait_idle(ysync + 4 * Y_WFRAG, 8);
    {
      const uint32_t nul = NULL0 + lane;
      const uint32_t a = opaque(lds0 + L_LIST + (w * 64 + lane) * 16);
#pragma unroll
      for (int i = 0; i < 5; ++i)
        if (i < 4 || w * 64 < (L_JUNK - L_LIST) / 16 - 2048) *lds_at<u32x4>(a + i * 8192) = u32x4{nul, nul, nul, nul};
    }
    lds_arrive(ysync + 4 * Y_ORD);
    lds_wait_idle(ysync + 4 * Y_TAB, BV_GATE);
    STAMP(2);
#pragma unroll
    for (int k = 0; k < 3; ++k) store_bv(rot(k), o[k]);
    asm volatile("s_waitcnt vmcnt(0)" ::: "memory");
    STAMP(3);
    if (lds_arrive_last(ysync + 4 * Y_BV, 8)) {
      SET_TAG(ep);
      if (lane == 0) store_tagged(ws + WS_FLAG + (g * 64 + range) * 128, 0, tlo, thi);
      poller = true;
    }
    if (!poller) __builtin_amdgcn_s_setprio(0);
    lds_wait_idle(ysync + 4 * Y_ORD, 16);
    setup();
  } else {
    __builtin_amdgcn_s_setprio(SORT_PRIO);
    const bool producer = b < NP;
    const uint32_t pb = producer ? CH : 0;
    const rsrc_t r_dst = make_rsrc(dst + b * pb, pb * 4), r_src = make_rsrc(src + b * pb, pb * 4);
    const rsrc_t r_ea = make_rsrc(eattr + b * pb, pb * 2);
    const u32x4 d4 = __builtin_amdgcn_raw_buffer_load_b128(r_dst, tid * 16, 0, 0);
    const uint32_t d1 = __builtin_amdgcn_raw_buffer_load_b32(r_dst, tid * 4, 8192, 0);
    const u32x4 s4 = __builtin_amdgcn_raw_buffer_load_b128(r_src, tid * 16, 0, 0);
    const u32x2 e4 = __builtin_amdgcn_raw_buffer_load_b64(r_ea, tid * 8, 0, 0);
    const uint32_t s1 = __builtin_amdgcn_raw_buffer_load_b32(r_src, tid * 4, 8192, 0);
    const uint32_t e1 = __builtin_amdgcn_raw_buffer_load_b16(r_ea, tid * 2, 4096, 0);
    const uint64_t ep = LOAD_EPOCH();
    u32x2 wcv = *(const u32x2*)(wc + g * 32 + (lane & 7) * 4);
    if (tid < L_ZEND / 16) *(u32x4*)(smem + tid * 16) = u32x4{0u, 0u, 0u, 0u};
    if (tid >= 512 - CHP / 8) {
      const uint32_t pad = (4 * SLOTS + (b & 7)) * 0x40004u;
      *(u32x4*)(smem + L_SD + (511 - tid) * 16) = u32x4{pad, pad, pad, pad};
    }
    STAMP(1);
    lds_barrier();
    STAMP(2);
    int phase = 0;
    if (producer) {
      const bool last = tid < CH - 2048;
      uint32_t d[5] = {d4[0], d4[1], d4[2], d4[3], d1}, s[5], rg[5], rk[5];
#pragma unroll
      for (int k = 0; k < 5; ++k) {
        asm("v_mul_hi_u32_u24 %0, 0xa79c7c, %1" : "=v"(rg[k]) : "v"(d[k]));
        rg[k] <<= 2;
        rk[k] = 0;
        if (k < 4 || last) rk[k] = atomicAdd((int*)(smem + L_PCNT + rg[k]), 2);
      }
      lds_arrive(ysync + 4 * Y_SORT);
      STAMP(5);
#pragma unroll
      for (int k = 0; k < 5; ++k)
        asm volatile("v_lshlrev_b32 %0, 4, %0\n\tv_mad_i32_i24 %0, %1, %2, %0" : "+v"(d[k]) : "v"(rg[k]), "s"(-4 * RN));
      const uint32_t o1 = opaque(b * (CHP * 4) + tid * 16);
      const uint32_t oh = (uint32_t)(WS_DL - WS_WORD) + b * (CHP * 2) - CHP * 4 + 8192 + tid * 16;
      const uint32_t o2 = opaque(tid < CHP / 4 - 512 ? o1 + 8192 : tid < CHP * 6 / 16 - 512 ? oh : 0x80000000u);
      const uint32_t a1 = opaque(lds0 + L_SW + tid * 16);
      const uint32_t a2 = opaque(tid < CHP * 6 / 16 - 512 ? a1 + 8192 : a1);
#pragma unroll
      for (int k = 0; k < 4; ++k) s[k] = opaque(__builtin_amdgcn_perm(e4[k >> 1], s4[k], (k & 1) ? 0x07060100u : 0x05040100u));
      s[4] = opaque(__builtin_amdgcn_perm(e1, s1, 0x05040100u));
#pragma unroll
      for (int k = 0; k < 5; ++k) {
        uint32_t qt;
        asm volatile("v_mul_hi_u32_u24 %1, 0xa7c5a, %2\n\tv_lshl_add_u32 %0, %1, 2, %0" : "+v"(d[k]), "=&v"(qt) : "v"(k < 4 ? s4[k] : s1));
      }
      lds_wait(ysync + 4 * Y_SORT, phase += 8);
      STAMP(3);
      const int c2 = (*(const int*)(smem + L_PCNT + lane * 4) + 6) & ~7;
      const int base2 = wave_scan(c2) - c2;
      constexpr int SB = L_SD - 60000;
      const int sbase = base2 + SB;
#pragma unroll
      for (int k = 0; k < 5; ++k) rk[k] += __builtin_amdgcn_ds_bpermute(rg[k], sbase);
#pragma unroll
      for (int k = 0; k < 5; ++k)
        if (k < 4 || last) {
          *lds_at<uint32_t>(rk[k] * 2 - (2 * SB - L_SW)) = s[k];
          *lds_at<uint16_t>(rk[k] + (L_SD - SB)) = (uint16_t)d[k];
        }
      lds_arrive(ysync + 4 * Y_SORT);
      STAMP(4);
      const rsrc_t r_both = make_rsrc(ws + WS_WORD, NP * CHP * 6);
      lds_wait(ysync + 4 * Y_SORT, phase += 8);
      const u32x4 y0 = *lds_at<u32x4>(a1);
      const u32x4 y1 = *lds_at<u32x4>(a2);
      __builtin_amdgcn_raw_buffer_store_b128(y0, r_both, o1, 0, SC1);
      __builtin_amdgcn_raw_buffer_store_b128(y1, r_both, o2, 0, SC1);
      if (HOLD != 0) lds_arrive(ysync + 4 * Y_STG);
      STAMP(6);
      asm volatile("s_waitcnt vmcnt(0)" ::: "memory");
      SET_TAG(ep);
      if (lds_arrive_last(ysync + 4 * Y_PUB, 8)) {
        store_tagged(ws + WS_OFF + b * OFFS + lane * 8, ((uint32_t)base2 >> 3) | ((uint32_t)c2 << 9), tlo, thi);
        if (HOLD != 0) lds_arrive(ysync + 4 * Y_STG);
      }
      STAMP(7);
    } else {
      SET_TAG(ep);
    }
    asm volatile("" : "+v"(tlo), "+v"(thi), "+v"(wcv));
    if (tid < 8) *(u32x2*)(smem + L_WC + tid * 8) = wcv;
    lds_arrive(ysync + 4 * Y_TAG);
    const int octl = oct_of(lane) << 2;
    if (wave < 3) {
      const char* pp = ws + WS_OFF + tid * OFFS + range * 8;
      if (producer) __builtin_amdgcn_s_sleep(DLY_P);
      else __builtin_amdgcn_s_sleep(DLY_N);
      if (HOLD == 3) lds_arrive(ysync + 4 * Y_STG);
      if (BV_GATE == 3) lds_arrive(ysync + 4 * Y_TAB);
      const uint32_t pc = poll_tagged<POLL_A>(pp, tid < NP, tlo, thi);
      *(uint32_t*)(smem + L_ORD + tid * 4) = (tid * (CHP / 4) + (pc & 0xfffu)) | ((pc >> 12) << 20);
      lds_arrive(ysync + 4 * Y_ENT);
    }
    lds_wait_idle(ysync + 4 * Y_ENT, 3);
    STAMP(8);
    uint32_t pq = *(const uint32_t*)(smem + L_ORD + (wave * SEG + lane) * 4);
    pq = lane < SEG ? pq : 0u;
    u32x8 pk = {0u, 0u, 0u, 0u, 0u, 0u, 0u, 0u};
#pragma unroll
    for (int i = 0; i < 5; ++i)
      pk[i] = __builtin_elementwise_sub_sat(*(const uint32_t*)(smem + L_ORD + (wave * SEG + (lane >> 4) + i * 4) * 4),
                                            (lane & 15) * 0xfffffu);
    const uint64_t lng = __builtin_amdgcn_ballot_w64(pq >= (17u << 20));
    if (lng != 0) {
      const uint64_t m2 = lng & (lng - 1);
      const uint32_t q0 = __builtin_amdgcn_readlane(pq, __builtin_ctzll(lng));
      const uint32_t q1 = m2 ? __builtin_amdgcn_readlane(pq, __builtin_ctzll(m2)) : 0u;
      pk[5] = __builtin_elementwise_sub_sat(lane < 32 ? q0 : q1, (16 + (lane & 31)) * 0xfffffu);
      if ((m2 & (m2 - 1)) != 0 || (q0 >> 20) > 48 || (q1 >> 20) > 48) sync[Y_SLOW] = 1;
    }
    u32x16 wd0, dl0;
    u32x8 wd1, dl1;
    asm volatile(
        "v_cmp_gt_u32 s[64:65], v80, %[thr]\n\t"
        "v_cmp_gt_u32 s[66:67], v81, %[thr]\n\t"
        "v_cmp_gt_u32 s[68:69], v82, %[thr]\n\t"
        "v_cmp_gt_u32 s[70:71], v83, %[thr]\n\t"
        "v_cmp_gt_u32 s[72:73], v84, %[thr]\n\t"
        "v_cmp_gt_u32 s[74:75], v85, %[thr]\n\t"
        "v_and_b32 v56, %[thr], v80\n\t"
        "v_and_b32 v60, %[thr], v81\n\t"
        "v_and_b32 v64, %[thr], v82\n\t"
        "v_and_b32 v68, %[thr], v83\n\t"
        "v_and_b32 v72, %[thr], v84\n\t"
        "v_and_b32 v76, %[thr], v85\n\t"
        "s_mov_b64 exec, s[64:65]\n\t"
        "buffer_load_dwordx2 v[58:59], v56, %[rh], 0 idxen sc1\n\t"
        "s_mov_b64 exec, s[66:67]\n\t"
        "buffer_load_dwordx2 v[62:63], v60, %[rh], 0 idxen sc1\n\t"
        "s_mov_b64 exec, s[68:69]\n\t"
        "buffer_load_dwordx2 v[66:67], v64, %[rh], 0 idxen sc1\n\t"
        "s_mov_b64 exec, s[70:71]\n\t"
        "buffer_load_dwordx2 v[70:71], v68, %[rh], 0 idxen sc1\n\t"
        "s_mov_b64 exec, s[72:73]\n\t"
        "buffer_load_dwordx2 v[74:75], v72, %[rh], 0 idxen sc1\n\t"
        "s_mov_b64 exec, s[74:75]\n\t"
        "buffer_load_dwordx2 v[78:79], v76, %[rh], 0 idxen sc1\n\t"
        "s_mov_b64 exec, 1\n\t"
        "ds_add_u32 %[gate], %[c32]\n\t"
        "s_mov_b64 exec, s[64:65]\n\t"
        "s_waitcnt vmcnt(5)\n\t"
        "v_and_b32 v56, 0xffff, v58\n\t"
        "v_lshrrev_b32 v57, 16, v58\n\t"
        "ds_add_u32 v56, %[c32]\n\t"
        "v_and_b32 v58, 0xffff, v59\n\t"
        "ds_add_u32 v57, %[c32]\n\t"
        "v_lshrrev_b32 v59, 16, v59\n\t"
        "ds_add_u32 v58, %[c32]\n\t"
        "ds_add_u32 v59, %[c32]\n\t"
        "s_mov_b64 exec, s[66:67]\n\t"
        "s_waitcnt vmcnt(4)\n\t"
        "v_and_b32 v60, 0xffff, v62\n\t"
        "v_lshrrev_b32 v61, 16, v62\n\t"
        "ds_add_u32 v60, %[c32]\n\t"
        "v_and_b32 v62, 0xffff, v63\n\t"
        "ds_add_u32 v61, %[c32]\n\t"
        "v_lshrrev_b32 v63, 16, v63\n\t"
        "ds_add_u32 v62, %[c32]\n\t"
        "ds_add_u32 v63, %[c32]\n\t"
        "s_mov_b64 exec, s[68:69]\n\t"
        "s_waitcnt vmcnt(3)\n\t"
        "v_and_b32 v64, 0xffff, v66\n\t"
        "v_lshrrev_b32 v65, 16, v66\n\t"
        "ds_add_u32 v64, %[c32]\n\t"
        "v_and_b32 v66, 0xffff, v67\n\t"
        "ds_add_u32 v65, %[c32]\n\t"
        "v_lshrrev_b32 v67, 16, v67\n\t"
        "ds_add_u32 v66, %[c32]\n\t"
        "ds_add_u32 v67, %[c32]\n\t"
        "s_mov_b64 exec, s[70:71]\n\t"
        "s_waitcnt vmcnt(2)\n\t"
        "v_and_b32 v68, 0xffff, v70\n\t"
        "v_lshrrev_b32 v69, 16, v70\n\t"
        "ds_add_u32 v68, %[c32]\n\t"
        "v_and_b32 v70, 0xffff, v71\n\t"
        "ds_add_u32 v69, %[c32]\n\t"
        "v_lshrrev_b32 v71, 16, v71\n\t"
        "ds_add_u32 v70, %[c32]\n\t"
        "ds_add_u32 v71, %[c32]\n\t"
        "s_mov_b64 exec, s[72:73]\n\t"
        "s_waitcnt vmcnt(1)\n\t"
        "v_and_b32 v72, 0xffff, v74\n\t"
        "v_lshrrev_b32 v73, 16, v74\n\t"
        "ds_add_u32 v72, %[c32]\n\t"
        "v_and_b32 v74, 0xffff, v75\n\t"
        "ds_add_u32 v73, %[c32]\n\t"
        "v_lshrrev_b32 v75, 16, v75\n\t"
        "ds_add_u32 v74, %[c32]\n\t"
        "ds_add_u32 v75, %[c32]\n\t"
        "s_mov_b64 exec, s[74:75]\n\t"
        "s_waitcnt vmcnt(0)\n\t"
        "v_and_b32 v76, 0xffff, v78\n\t"
        "v_lshrrev_b32 v77, 16, v78\n\t"
        "ds_add_u32 v76, %[c32]\n\t"
        "v_and_b32 v78, 0xffff, v79\n\t"
        "ds_add_u32 v77, %[c32]\n\t"
        "v_lshrrev_b32 v79, 16, v79\n\t"
        "ds_add_u32 v78, %[c32]\n\t"
        "ds_add_u32 v79, %[c32]\n\t"
        "s_mov_b64 exec, -1\n\t"
        : "={v[56:71]}"(dl0), "={v[72:79]}"(dl1)
        : "{v[80:87]}"(pk), [thr] "{v88}"(0xfffffu), [c32] "{v89}"(32), [gate] "{v90}"(ysync + 4 * Y_TAB),
          [rh] "{s[80:83]}"(make_srd(ws + WS_DL, NP * CHP / 4, 8))
        : "memory", SEG_SGPRS);
    auto load_words = [&]() {
      asm volatile(
        "v_cmp_gt_u32 s[64:65], v80, %[thr]\n\t"
        "v_cmp_gt_u32 s[66:67], v81, %[thr]\n\t"
        "v_cmp_gt_u32 s[68:69], v82, %[thr]\n\t"
        "v_cmp_gt_u32 s[70:71], v83, %[thr]\n\t"
        "v_cmp_gt_u32 s[72:73], v84, %[thr]\n\t"
        "v_cmp_gt_u32 s[74:75], v85, %[thr]\n\t"
        "v_and_b32 v32, %[thr], v80\n\t"
        "v_and_b32 v36, %[thr], v81\n\t"
        "v_and_b32 v40, %[thr], v82\n\t"
        "v_and_b32 v44, %[thr], v83\n\t"
        "v_and_b32 v48, %[thr], v84\n\t"
        "v_and_b32 v52, %[thr], v85\n\t"
        "s_mov_b64 exec, s[64:65]\n\t"
        "buffer_load_dwordx4 v[32:35], v32, %[rw], 0 idxen sc1\n\t"
        "s_mov_b64 exec, s[66:67]\n\t"
        "buffer_load_dwordx4 v[36:39], v36, %[rw], 0 idxen sc1\n\t"
        "s_mov_b64 exec, s[68:69]\n\t"
        "buffer_load_dwordx4 v[40:43], v40, %[rw], 0 idxen sc1\n\t"
        "s_mov_b64 exec, s[70:71]\n\t"
        "buffer_load_dwordx4 v[44:47], v44, %[rw], 0 idxen sc1\n\t"
        "s_mov_b64 exec, s[72:73]\n\t"
        "buffer_load_dwordx4 v[48:51], v48, %[rw], 0 idxen sc1\n\t"
        "s_mov_b64 exec, s[74:75]\n\t"
        "buffer_load_dwordx4 v[52:55], v52, %[rw], 0 idxen sc1\n\t"
        "s_mov_b64 exec, -1\n\t"
        : "={v[32:47]}"(wd0), "={v[48:55]}"(wd1)
        : "{v[80:87]}"(pk), [thr] "{v88}"(0xfffffu), [rw] "{s[76:79]}"(make_srd(ws + WS_WORD, NP * CHP / 4, 16))
        : "memory", SEG_SGPRS);
    };
    STAMP(9);
    lds_arrive(ysync + 4 * Y_SORT);
    if (WORDS_AT == 0) load_words();
    lds_wait(ysync + 4 * Y_SORT, phase += 8);
    int key = 0, rk = 0;
    u32x4 c4 = {0u, 0u, 0u, 0u};
    if (tid < SLOTS) {
      c4 = *(const u32x4*)(smem + L_CNT + tid * 16);
      c4[1] += c4[0];
      c4[2] += c4[1];
      c4[3] += c4[2];
      const int np = (int)(c4[3] + 32) >> 6;
      key = (63 - (np < 63 ? np : 63)) << 2;
      rk = atomicAdd((int*)(smem + L_HIST + key), 1);
    }
    lds_arrive(ysync + 4 * Y_SORT);
    lds_wait(ysync + 4 * Y_SORT, phase += 8);
    const int h = *(const int*)(smem + L_HIST + lane * 4);
    const int slow0 = sync[Y_SLOW];
    const int incl = wave_scan(h), excl = incl - h;
    const int f0 = (excl + 7) >> 3, f1 = (incl + 7) >> 3;
    const int ko = wave_scan_max(__builtin_amdgcn_ds_permute((f0 < f1 ? f0 : 63) << 2, lane));
    const int pos = __builtin_amdgcn_ds_bpermute(key, excl) + rk;
    const int npj = __builtin_amdgcn_ds_bpermute(octl, lane < 49 ? (ko < 63 ? 63 - ko : 1) : 0);
    const int incl2 = wave_scan(npj);
    const int st = __builtin_amdgcn_ds_bpermute(turn_of(pos >> 3) << 2, incl2 - npj);
    if (tid < SLOTS) {
      const uint32_t base = lds0 + L_LIST + st * 64 + (pos & 7) * 4;
      *(u32x4*)(smem + L_CNT + tid * 16) = u32x4{base, base + c4[0], base + c4[1], base + c4[2]};
      *(uint32_t*)(smem + L_ORD + pos * 4) = (uint32_t)(tid * 256 + (tid & 15) * 16);
    } else if (tid < SLOTS + 8) {
      cnt[3 * SLOTS + tid] = (int)lds0 + L_JUNK + (tid & 7) * 4;
    }
    const bool slow = __builtin_amdgcn_readfirstlane(h | slow0) != 0 || __builtin_amdgcn_readlane(incl2, 63) > LIST_STEPS;
    if (wave == 7) {
      *(int*)(smem + L_TURN + lane * 4) = ((incl2 - npj) << 8) | npj;
      if (slow) sync[Y_SLOW] = 1;
    }
    lds_arrive(ysync + 4 * Y_ORD);
    STAMP(10);
    if (WORDS_AT == 1) load_words();
    const uint32_t thr = slow ? 0xffffffffu : 0xfffffu;
    {
      uint32_t tmp;
      asm volatile(
        "v_cmp_gt_u32 s[64:65], v80, %[thr]\n\t"
        "v_cmp_gt_u32 s[66:67], v81, %[thr]\n\t"
        "v_cmp_gt_u32 s[68:69], v82, %[thr]\n\t"
        "v_cmp_gt_u32 s[70:71], v83, %[thr]\n\t"
        "v_cmp_gt_u32 s[72:73], v84, %[thr]\n\t"
        "v_cmp_gt_u32 s[74:75], v85, %[thr]\n\t"
        "Lw%=:\n\t"
        "ds_read_b32 %[tmp], %[ctr]\n\t"
        "s_waitcnt lgkmcnt(0)\n\t"
        "v_cmp_gt_i32 vcc, 16, %[tmp]\n\t"
        "s_cbranch_vccnz Lw%=\n\t"
        "s_mov_b64 exec, s[64:65]\n\t"
        "ds_add_rtn_u32 v56, v56, %[c32]\n\t"
        "ds_add_rtn_u32 v57, v57, %[c32]\n\t"
        "ds_add_rtn_u32 v58, v58, %[c32]\n\t"
        "ds_add_rtn_u32 v59, v59, %[c32]\n\t"
        "s_mov_b64 exec, s[66:67]\n\t"
        "ds_add_rtn_u32 v60, v60, %[c32]\n\t"
        "ds_add_rtn_u32 v61, v61, %[c32]\n\t"
        "ds_add_rtn_u32 v62, v62, %[c32]\n\t"
        "ds_add_rtn_u32 v63, v63, %[c32]\n\t"
        "s_mov_b64 exec, s[68:69]\n\t"
        "ds_add_rtn_u32 v64, v64, %[c32]\n\t"
        "ds_add_rtn_u32 v65, v65, %[c32]\n\t"
        "ds_add_rtn_u32 v66, v66, %[c32]\n\t"
        "ds_add_rtn_u32 v67, v67, %[c32]\n\t"
        "s_mov_b64 exec, s[70:71]\n\t"
        "ds_add_rtn_u32 v68, v68, %[c32]\n\t"
        "ds_add_rtn_u32 v69, v69, %[c32]\n\t"
        "ds_add_rtn_u32 v70, v70, %[c32]\n\t"
        "ds_add_rtn_u32 v71, v71, %[c32]\n\t"
        "s_mov_b64 exec, s[72:73]\n\t"
        "ds_add_rtn_u32 v72, v72, %[c32]\n\t"
        "ds_add_rtn_u32 v73, v73, %[c32]\n\t"
        "ds_add_rtn_u32 v74, v74, %[c32]\n\t"
        "ds_add_rtn_u32 v75, v75, %[c32]\n\t"
        "s_mov_b64 exec, s[74:75]\n\t"
        "ds_add_rtn_u32 v76, v76, %[c32]\n\t"
        "ds_add_rtn_u32 v77, v77, %[c32]\n\t"
        "ds_add_rtn_u32 v78, v78, %[c32]\n\t"
        "ds_add_rtn_u32 v79, v79, %[c32]\n\t"
        "s_mov_b64 exec, -1\n\t"
        : "+{v[56:71]}"(dl0), "+{v[72:79]}"(dl1), [tmp] "=&{v90}"(tmp)
        : "{v[80:87]}"(pk), [thr] "{v88}"(thr), [ctr] "{v91}"(ysync + 4 * Y_ORD), [c32] "{v89}"(32)
        : "memory", "vcc", SEG_SGPRS);
    }
    setup();
    asm volatile(
        "v_cmp_gt_u32 s[64:65], v80, %[thr]\n\t"
        "v_cmp_gt_u32 s[66:67], v81, %[thr]\n\t"
        "v_cmp_gt_u32 s[68:69], v82, %[thr]\n\t"
        "v_cmp_gt_u32 s[70:71], v83, %[thr]\n\t"
        "v_cmp_gt_u32 s[72:73], v84, %[thr]\n\t"
        "v_cmp_gt_u32 s[74:75], v85, %[thr]\n\t"
        "s_mov_b64 exec, s[64:65]\n\t"
        "s_waitcnt vmcnt(5) lgkmcnt(0)\n\t"
        "ds_write_b32 v56, v32\n\t"
        "ds_write_b32 v57, v33\n\t"
        "ds_write_b32 v58, v34\n\t"
        "ds_write_b32 v59, v35\n\t"
        "s_mov_b64 exec, s[66:67]\n\t"
        "s_waitcnt vmcnt(4)\n\t"
        "ds_write_b32 v60, v36\n\t"
        "ds_write_b32 v61, v37\n\t"
        "ds_write_b32 v62, v38\n\t"
        "ds_write_b32 v63, v39\n\t"
        "s_mov_b64 exec, s[68:69]\n\t"
        "s_waitcnt vmcnt(3)\n\t"
        "ds_write_b32 v64, v40\n\t"
        "ds_write_b32 v65, v41\n\t"
        "ds_write_b32 v66, v42\n\t"
        "ds_write_b32 v67, v43\n\t"
        "s_mov_b64 exec, s[70:71]\n\t"
        "s_waitcnt vmcnt(2)\n\t"
        "ds_write_b32 v68, v44\n\t"
        "ds_write_b32 v69, v45\n\t"
        "ds_write_b32 v70, v46\n\t"
        "ds_write_b32 v71, v47\n\t"
        "s_mov_b64 exec, s[72:73]\n\t"
        "s_waitcnt vmcnt(1)\n\t"
        "ds_write_b32 v72, v48\n\t"
        "ds_write_b32 v73, v49\n\t"
        "ds_write_b32 v74, v50\n\t"
        "ds_write_b32 v75, v51\n\t"
        "s_mov_b64 exec, s[74:75]\n\t"
        "s_waitcnt vmcnt(0)\n\t"
        "ds_write_b32 v76, v52\n\t"
        "ds_write_b32 v77, v53\n\t"
        "ds_write_b32 v78, v54\n\t"
        "ds_write_b32 v79, v55\n\t"
        "s_mov_b64 exec, -1\n\t"
        :
        : "{v[56:71]}"(dl0), "{v[72:79]}"(dl1), "{v[32:47]}"(wd0), "{v[48:55]}"(wd1), "{v[80:87]}"(pk), [thr] "{v88}"(thr)
        : "memory", SEG_SGPRS);
    STAMP(11);
  }

  u32x4 np4;
#pragma unroll
  for (int i = 0; i < 4; ++i) np4[i] = __builtin_amdgcn_readfirstlane(tq[i]) & 0xffu;
  uint32_t rem = np4[0] + np4[1] + np4[2] + np4[3];
  const uint64_t tb = (uint64_t)tab, ob = (uint64_t)(out + (size_t)node0 * 128 + g * 32);
  u32x4 srd_tab = {uni((uint32_t)tb), uni((uint32_t)(tb >> 32) | (128u << 16)), (uint32_t)TAB_RECORDS, 0x00020000u};
  const u32x4 srd_out = {uni((uint32_t)ob), uni((uint32_t)(ob >> 32)), uni(nvalid * 256 - g * 64), 0x00020000u};
  const uint32_t coff = opaque(c * 16);
  const bool take_slow = __builtin_amdgcn_readfirstlane(slow_path) != 0;
  __builtin_amdgcn_s_setprio(0);
  if (poller) {
    poll_tagged<POLL_B>(ws + WS_FLAG + (g * 64 + ln) * 128, true, tlo, thi);
    const u32x4 srd_pf = {srd_tab[0], srd_tab[1] & 0xffffu, NN * 128, 0x00020000u};
#pragma unroll
    for (int k = 0; k < PREFETCH; ++k) {
      uint32_t keep;
      asm volatile("s_mov_b32 %0, m0\n\ts_mov_b32 m0, %4\n\ts_nop 3\n\tbuffer_load_dword %1, %2, %3 offen sc1 lds\n\ts_mov_b32 m0, %0"
                   : "=&s"(keep) : "v"(ln * 128), "s"(srd_pf),
                   "s"(uni(((range & (MAP == 0 ? 31 : 15)) * PREFETCH + k) * 8192)), "s"(lds0 + L_JUNK) : "memory");
    }
  }
  tlo = uni(tlo);
  thi = uni(thi);
  lds_barrier();
  STAMP(12);
  auto au_ptr = [&](int n) { return smem + L_X + ((n * 256 + (n & 15) * 16) ^ k_au); };
  auto x_ptr = [&](int n) { return smem + L_X + ((n * 256 + (n & 15) * 16) ^ k_x); };

  if (__builtin_expect(take_slow, 0)) {
    const rsrc_t r_bv = make_rsrc(tab, TROWS * 128);
    float* den = (float*)(smem + L_LIST);
    float* num = den + 131 * 32;
#pragma unroll 1
    for (int n0 = 0; n0 < RN; n0 += 131) {
      for (int i = wave * 64 + ln; i < 131 * 16; i += 1024) *(f32x4*)(smem + L_LIST + i * 16) = zero4;
      lds_barrier();
      for (int p = 0; p < NP; ++p) {
        const uint32_t o = (uint32_t)__hip_atomic_load((const uint64_t*)(ws + WS_OFF + p * OFFS + range * 8),
                                                       __ATOMIC_RELAXED, __HIP_MEMORY_SCOPE_AGENT);
        const int n = __builtin_amdgcn_readfirstlane((o >> 12) & 0xfffu) * 4, st = __builtin_amdgcn_readfirstlane(o & 0xfffu) * 4;
        for (int i0 = ((wave + p) & 15) * 8; i0 < n; i0 += 128) {
          const int i = i0 + slot < n ? i0 + slot : 0;
          const uint32_t ww = __builtin_amdgcn_raw_buffer_load_b32(r_word, i * 4, (p * CHP + st) * 4, SC1);
          const int dl = (int)__builtin_amdgcn_raw_buffer_load_b16(r_dl, i * 2, (p * CHP + st) * 2, SC1) >> 4;
          const u32x4 gv = __builtin_amdgcn_raw_buffer_load_b128(r_bv, (ww & 0xffffu) * 128u + c * 16, 0, SC1);
          const u32x2 av = *(const u32x2*)au_ptr(dl);
          const float e = __builtin_bit_cast(float, ww & 0xffff0000u);
          const f32x4 t = unpack_add(ident, gv[0], gv[1], unpack_add(ident, av[0], av[1], cw * e));
          const f32x4 v = unpack_add(ident, gv[2], gv[3], zero4);
          if (i0 + slot < n && dl >= n0 && dl < n0 + 131 && dl < SLOTS) {
#pragma unroll
            for (int k = 0; k < 4; ++k) {
              const float sg = __builtin_amdgcn_rcpf(1.f + __builtin_amdgcn_exp2f(t[k]));
              atomicAdd(&den[(dl - n0) * 32 + c * 4 + k], sg);
              atomicAdd(&num[(dl - n0) * 32 + c * 4 + k], sg * v[k]);
            }
          }
        }
      }
      lds_barrier();
      for (int o = wave; o < 17; o += 16) {
        const bool ok = o * 8 + slot < 131 && n0 + o * 8 + slot < nvalid;
        const int nl = ok ? o * 8 + slot : 0, n = n0 + nl;
        const u32x4 av = *(const u32x4*)au_ptr(n);
        const u32x2 xv = *(const u32x2*)x_ptr(n);
        const f32x4 dn = *(const f32x4*)(den + nl * 32 + c * 4), nm = *(const f32x4*)(num + nl * 32 + c * 4);
        f32x4 z = unpack_add(ident, av[2], av[3], zero4);
#pragma unroll
        for (int k = 0; k < 4; ++k) z[k] = fmaxf(fmaf(nm[k], __builtin_amdgcn_rcpf(dn[k] + 1e-6f), z[k]), 0.f);
        z = unpack_add(ident, xv[0], xv[1], z);
        const u32x2 ov = {pack_bf16(z[0], z[1]), pack_bf16(z[2], z[3])};
        if (ok) *(u32x2*)(out + (size_t)(node0 + n) * 128 + g * 32 + c * 4) = ov;
      }
      lds_barrier();
    }
  } else

  {
    STAMP(15);
    asm volatile(
      "s_nop 4\n\t"
      "s_getpc_b64 s[60:61]\n\t"
      "Lpc%=:\n\t"
      "s_setprio 2\n\t"
      "s_cmp_eq_u32 s59, 0\n\t"
      "s_cbranch_scc1 Lpr0%=\n\t"
      "s_setprio 3\n\t"
      "Lpr0%=:\n\t"
      "ds_read_u16 v40, v16 offset:0\n\t"
      "ds_read_u16 v41, v16 offset:32\n\t"
      "ds_read_u16 v42, v16 offset:64\n\t"
      "ds_read_u16 v43, v16 offset:96\n\t"
      "ds_read_u16 v44, v16 offset:128\n\t"
      "ds_read_u16 v45, v16 offset:160\n\t"
      "ds_read_u16 v46, v16 offset:192\n\t"
      "ds_read_u16 v47, v16 offset:224\n\t"
      "v_mov_b32 v10, 0\n\t"
      "v_mov_b32 v11, 0\n\t"
      "v_mov_b32 v15, v13\n\t"
      "v_mov_b32 v120, 0xc2700000\n\t"
      "v_mov_b32 v121, 0xc2700000\n\t"
      "v_mov_b32 v122, 0xc2700000\n\t"
      "v_mov_b32 v123, 0xc2700000\n\t"
      "s_mov_b32 s48, 0x21800000\n\t"
      "s_mov_b32 s49, 0x21800000\n\t"
      "s_mov_b32 s62, 0x538637bd\n\t"
      "s_mov_b32 s63, 0x538637bd\n\t"
      "v_mfma_f32_4x4x4_16b_bf16 v[6:9], v[0:1], v[96:97], v[120:123]\n\t"
      "s_waitcnt lgkmcnt(0)\n\t"
      "v_mov_b32 v12, v40\n\t"
      "v_mov_b32 v14, v41\n\t"
      "s_sub_u32 s51, s51, 1\n\t"
      "s_cselect_b32 s42, 0, s42\n\t"
      "buffer_load_dwordx4 v[64:67], v[12:13], s[40:43], 0 idxen offen sc1\n\t"
      "buffer_load_dwordx4 v[68:71], v[14:15], s[40:43], 0 idxen offen sc1\n\t"
      "v_mov_b32 v12, v42\n\t"
      "v_mov_b32 v14, v43\n\t"
      "s_sub_u32 s51, s51, 1\n\t"
      "s_cselect_b32 s42, 0, s42\n\t"
      "buffer_load_dwordx4 v[72:75], v[12:13], s[40:43], 0 idxen offen sc1\n\t"
      "buffer_load_dwordx4 v[76:79], v[14:15], s[40:43], 0 idxen offen sc1\n\t"
      "v_mov_b32 v12, v44\n\t"
      "v_mov_b32 v14, v45\n\t"
      "s_sub_u32 s51, s51, 1\n\t"
      "s_cselect_b32 s42, 0, s42\n\t"
      "buffer_load_dwordx4 v[80:83], v[12:13], s[40:43], 0 idxen offen sc1\n\t"
      "buffer_load_dwordx4 v[84:87], v[14:15], s[40:43], 0 idxen offen sc1\n\t"
      "v_mov_b32 v12, v46\n\t"
      "v_mov_b32 v14, v47\n\t"
      "s_sub_u32 s51, s51, 1\n\t"
      "s_cselect_b32 s42, 0, s42\n\t"
      "buffer_load_dwordx4 v[88:91], v[12:13], s[40:43], 0 idxen offen sc1\n\t"
      "buffer_load_dwordx4 v[92:95], v[14:15], s[40:43], 0 idxen offen sc1\n\t"
      "ds_read_u16_d16_hi v10, v16 offset:2\n\t"
      "ds_read_u16_d16_hi v11, v16 offset:34\n\t"
      "ds_read_u16 v12, v16 offset:256\n\t"
      "ds_read_u16 v14, v16 offset:288\n\t"
      "v_mov_b32 v56, s62\n\t"
      "v_mov_b32 v60, 0\n\t"
      "v_mov_b32 v57, s62\n\t"
      "v_mov_b32 v61, 0\n\t"
      "v_mov_b32 v58, s62\n\t"
      "v_mov_b32 v62, 0\n\t"
      "v_mov_b32 v59, s62\n\t"
      "v_mov_b32 v63, 0\n\t"
      "s_waitcnt vmcnt(6)\n\t"
      "v_mfma_f32_4x4x4_16b_bf16 v[24:27], v[0:1], v[64:65], v[6:9]\n\t"
      "v_mfma_f32_4x4x4_16b_bf16 v[28:31], v[0:1], v[68:69], v[6:9]\n\t"
      "s_waitcnt lgkmcnt(2)\n\t"
      "s_nop 4\n\t"
      "v_pk_fma_f32 v[24:25], v[2:3], v[10:11], v[24:25] op_sel_hi:[1,0,1]\n\t"
      "v_pk_fma_f32 v[26:27], v[4:5], v[10:11], v[26:27] op_sel_hi:[1,0,1]\n\t"
      "v_pk_fma_f32 v[28:29], v[2:3], v[10:11], v[28:29] op_sel:[0,1,0] op_sel_hi:[1,1,1]\n\t"
      "v_pk_fma_f32 v[30:31], v[4:5], v[10:11], v[30:31] op_sel:[0,1,0] op_sel_hi:[1,1,1]\n\t"
      "ds_read_u16_d16_hi v10, v16 offset:66\n\t"
      "ds_read_u16_d16_hi v11, v16 offset:98\n\t"
      "s_add_u32 s52, s60, LA0%=-Lpc%=\n\t"
      "s_addc_u32 s53, s61, 0\n\t"
      "s_sub_u32 s50, s56, 2\n\t"
      "s_cbranch_scc0 Lstep0%=\n\t"
      "s_swappc_b64 s[54:55], s[52:53]\n\t"
      "Lstep0%=:\n\t"
      "s_waitcnt vmcnt(4) lgkmcnt(2)\n\t"
      "v_mfma_f32_4x4x4_16b_bf16 v[40:43], v[0:1], v[66:67], 0\n\t"
      "v_exp_f32_e64 v24, v24 clamp\n\t"
      "v_mfma_f32_4x4x4_16b_bf16 v[44:47], v[0:1], v[70:71], 0\n\t"
      "v_exp_f32_e64 v25, v25 clamp\n\t"
      "v_mfma_f32_4x4x4_16b_bf16 v[32:35], v[0:1], v[72:73], v[6:9]\n\t"
      "v_exp_f32_e64 v26, v26 clamp\n\t"
      "v_mfma_f32_4x4x4_16b_bf16 v[36:39], v[0:1], v[76:77], v[6:9]\n\t"
      "v_exp_f32_e64 v27, v27 clamp\n\t"
      "s_sub_u32 s51, s51, 1\n\t"
      "v_exp_f32_e64 v28, v28 clamp\n\t"
      "s_cselect_b32 s42, 0, s42\n\t"
      "v_exp_f32_e64 v29, v29 clamp\n\t"
      "buffer_load_dwordx4 v[64:67], v[12:13], s[40:43], 0 idxen offen sc1\n\t"
      "v_exp_f32_e64 v30, v30 clamp\n\t"
      "buffer_load_dwordx4 v[68:71], v[14:15], s[40:43], 0 idxen offen sc1\n\t"
      "v_exp_f32_e64 v31, v31 clamp\n\t"
      "s_sub_u32 s50, s50, 1\n\t"
      "v_pk_add_f32 v[24:25], v[24:25], s[48:49]\n\t"
      "v_pk_add_f32 v[28:29], v[28:29], s[48:49]\n\t"
      "v_pk_add_f32 v[26:27], v[26:27], s[48:49]\n\t"
      "v_pk_add_f32 v[30:31], v[30:31], s[48:49]\n\t"
      "v_pk_mul_f32 v[48:49], v[24:25], v[28:29]\n\t"
      "v_pk_mul_f32 v[50:51], v[26:27], v[30:31]\n\t"
      "v_rcp_f32_e32 v48, v48\n\t"
      "ds_read_u16 v12, v16 offset:320\n\t"
      "v_rcp_f32_e32 v49, v49\n\t"
      "ds_read_u16 v14, v16 offset:352\n\t"
      "v_rcp_f32_e32 v50, v50\n\t"
      "s_waitcnt lgkmcnt(2)\n\t"
      "v_rcp_f32_e32 v51, v51\n\t"
      "v_pk_fma_f32 v[32:33], v[2:3], v[10:11], v[32:33] op_sel_hi:[1,0,1]\n\t"
      "v_pk_fma_f32 v[34:35], v[4:5], v[10:11], v[34:35] op_sel_hi:[1,0,1]\n\t"
      "v_pk_fma_f32 v[36:37], v[2:3], v[10:11], v[36:37] op_sel:[0,1,0] op_sel_hi:[1,1,1]\n\t"
      "v_pk_fma_f32 v[38:39], v[4:5], v[10:11], v[38:39] op_sel:[0,1,0] op_sel_hi:[1,1,1]\n\t"
      "ds_read_u16_d16_hi v10, v16 offset:130\n\t"
      "ds_read_u16_d16_hi v11, v16 offset:162\n\t"
      "v_pk_add_f32 v[52:53], v[24:25], v[28:29]\n\t"
      "v_pk_add_f32 v[54:55], v[26:27], v[30:31]\n\t"
      "v_pk_mul_f32 v[44:45], v[44:45], v[24:25]\n\t"
      "v_pk_mul_f32 v[46:47], v[46:47], v[26:27]\n\t"
      "v_pk_fma_f32 v[40:41], v[40:41], v[28:29], v[44:45]\n\t"
      "v_pk_fma_f32 v[42:43], v[42:43], v[30:31], v[46:47]\n\t"
      "v_pk_fma_f32 v[56:57], v[52:53], v[48:49], v[56:57]\n\t"
      "v_pk_fma_f32 v[58:59], v[54:55], v[50:51], v[58:59]\n\t"
      "v_pk_fma_f32 v[60:61], v[40:41], v[48:49], v[60:61]\n\t"
      "v_pk_fma_f32 v[62:63], v[42:43], v[50:51], v[62:63]\n\t"
      "s_cbranch_scc1 Lstub0%=\n\t"
      "Lstep1%=:\n\t"
      "s_waitcnt vmcnt(4) lgkmcnt(2)\n\t"
      "v_mfma_f32_4x4x4_16b_bf16 v[40:43], v[0:1], v[74:75], 0\n\t"
      "v_exp_f32_e64 v32, v32 clamp\n\t"
      "v_mfma_f32_4x4x4_16b_bf16 v[44:47], v[0:1], v[78:79], 0\n\t"
      "v_exp_f32_e64 v33, v33 clamp\n\t"
      "v_mfma_f32_4x4x4_16b_bf16 v[24:27], v[0:1], v[80:81], v[6:9]\n\t"
      "v_exp_f32_e64 v34, v34 clamp\n\t"
      "v_mfma_f32_4x4x4_16b_bf16 v[28:31], v[0:1], v[84:85], v[6:9]\n\t"
      "v_exp_f32_e64 v35, v35 clamp\n\t"
      "s_sub_u32 s51, s51, 1\n\t"
      "v_exp_f32_e64 v36, v36 clamp\n\t"
      "s_cselect_b32 s42, 0, s42\n\t"
      "v_exp_f32_e64 v37, v37 clamp\n\t"
      "buffer_load_dwordx4 v[72:75], v[12:13], s[40:43], 0 idxen offen sc1\n\t"
      "v_exp_f32_e64 v38, v38 clamp\n\t"
      "buffer_load_dwordx4 v[76:79], v[14:15], s[40:43], 0 idxen offen sc1\n\t"
      "v_exp_f32_e64 v39, v39 clamp\n\t"
      "s_sub_u32 s50, s50, 1\n\t"
      "v_pk_add_f32 v[32:33], v[32:33], s[48:49]\n\t"
      "v_pk_add_f32 v[36:37], v[36:37], s[48:49]\n\t"
      "v_pk_add_f32 v[34:35], v[34:35], s[48:49]\n\t"
      "v_pk_add_f32 v[38:39], v[38:39], s[48:49]\n\t"
      "v_pk_mul_f32 v[48:49], v[32:33], v[36:37]\n\t"
      "v_pk_mul_f32 v[50:51], v[34:35], v[38:39]\n\t"
      "v_rcp_f32_e32 v48, v48\n\t"
      "ds_read_u16 v12, v16 offset:384\n\t"
      "v_rcp_f32_e32 v49, v49\n\t"
      "ds_read_u16 v14, v16 offset:416\n\t"
      "v_rcp_f32_e32 v50, v50\n\t"
      "s_waitcnt lgkmcnt(2)\n\t"
      "v_rcp_f32_e32 v51, v51\n\t"
      "v_pk_fma_f32 v[24:25], v[2:3], v[10:11], v[24:25] op_sel_hi:[1,0,1]\n\t"
      "v_pk_fma_f32 v[26:27], v[4:5], v[10:11], v[26:27] op_sel_hi:[1,0,1]\n\t"
      "v_pk_fma_f32 v[28:29], v[2:3], v[10:11], v[28:29] op_sel:[0,1,0] op_sel_hi:[1,1,1]\n\t"
      "v_pk_fma_f32 v[30:31], v[4:5], v[10:11], v[30:31] op_sel:[0,1,0] op_sel_hi:[1,1,1]\n\t"
      "ds_read_u16_d16_hi v10, v16 offset:194\n\t"
      "ds_read_u16_d16_hi v11, v16 offset:226\n\t"
      "v_pk_add_f32 v[52:53], v[32:33], v[36:37]\n\t"
      "v_pk_add_f32 v[54:55], v[34:35], v[38:39]\n\t"
      "v_pk_mul_f32 v[44:45], v[44:45], v[32:33]\n\t"
      "v_pk_mul_f32 v[46:47], v[46:47], v[34:35]\n\t"
      "v_pk_fma_f32 v[40:41], v[40:41], v[36:37], v[44:45]\n\t"
      "v_pk_fma_f32 v[42:43], v[42:43], v[38:39], v[46:47]\n\t"
      "v_pk_fma_f32 v[56:57], v[52:53], v[48:49], v[56:57]\n\t"
      "v_pk_fma_f32 v[58:59], v[54:55], v[50:51], v[58:59]\n\t"
      "v_pk_fma_f32 v[60:61], v[40:41], v[48:49], v[60:61]\n\t"
      "v_pk_fma_f32 v[62:63], v[42:43], v[50:51], v[62:63]\n\t"
      "s_cbranch_scc1 Lstub1%=\n\t"
      "Lstep2%=:\n\t"
      "s_waitcnt vmcnt(4) lgkmcnt(2)\n\t"
      "v_mfma_f32_4x4x4_16b_bf16 v[40:43], v[0:1], v[82:83], 0\n\t"
      "v_exp_f32_e64 v24, v24 clamp\n\t"
      "v_mfma_f32_4x4x4_16b_bf16 v[44:47], v[0:1], v[86:87], 0\n\t"
      "v_exp_f32_e64 v25, v25 clamp\n\t"
      "v_mfma_f32_4x4x4_16b_bf16 v[32:35], v[0:1], v[88:89], v[6:9]\n\t"
      "v_exp_f32_e64 v26, v26 clamp\n\t"
      "v_mfma_f32_4x4x4_16b_bf16 v[36:39], v[0:1], v[92:93], v[6:9]\n\t"
      "v_exp_f32_e64 v27, v27 clamp\n\t"
      "s_sub_u32 s51, s51, 1\n\t"
      "v_exp_f32_e64 v28, v28 clamp\n\t"
      "s_cselect_b32 s42, 0, s42\n\t"
      "v_exp_f32_e64 v29, v29 clamp\n\t"
      "buffer_load_dwordx4 v[80:83], v[12:13], s[40:43], 0 idxen offen sc1\n\t"
      "v_exp_f32_e64 v30, v30 clamp\n\t"
      "buffer_load_dwordx4 v[84:87], v[14:15], s[40:43], 0 idxen offen sc1\n\t"
      "v_exp_f32_e64 v31, v31 clamp\n\t"
      "s_sub_u32 s50, s50, 1\n\t"
      "v_pk_add_f32 v[24:25], v[24:25], s[48:49]\n\t"
      "v_pk_add_f32 v[28:29], v[28:29], s[48:49]\n\t"
      "v_pk_add_f32 v[26:27], v[26:27], s[48:49]\n\t"
      "v_pk_add_f32 v[30:31], v[30:31], s[48:49]\n\t"
      "v_pk_mul_f32 v[48:49], v[24:25], v[28:29]\n\t"
      "v_pk_mul_f32 v[50:51], v[26:27], v[30:31]\n\t"
      "v_rcp_f32_e32 v48, v48\n\t"
      "ds_read_u16 v12, v16 offset:448\n\t"
      "v_rcp_f32_e32 v49, v49\n\t"
      "ds_read_u16 v14, v16 offset:480\n\t"
      "v_rcp_f32_e32 v50, v50\n\t"
      "s_waitcnt lgkmcnt(2)\n\t"
      "v_rcp_f32_e32 v51, v51\n\t"
      "v_pk_fma_f32 v[32:33], v[2:3], v[10:11], v[32:33] op_sel_hi:[1,0,1]\n\t"
      "v_pk_fma_f32 v[34:35], v[4:5], v[10:11], v[34:35] op_sel_hi:[1,0,1]\n\t"
      "v_pk_fma_f32 v[36:37], v[2:3], v[10:11], v[36:37] op_sel:[0,1,0] op_sel_hi:[1,1,1]\n\t"
      "v_pk_fma_f32 v[38:39], v[4:5], v[10:11], v[38:39] op_sel:[0,1,0] op_sel_hi:[1,1,1]\n\t"
      "ds_read_u16_d16_hi v10, v16 offset:258\n\t"
      "ds_read_u16_d16_hi v11, v16 offset:290\n\t"
      "v_pk_add_f32 v[52:53], v[24:25], v[28:29]\n\t"
      "v_pk_add_f32 v[54:55], v[26:27], v[30:31]\n\t"
      "v_pk_mul_f32 v[44:45], v[44:45], v[24:25]\n\t"
      "v_pk_mul_f32 v[46:47], v[46:47], v[26:27]\n\t"
      "v_pk_fma_f32 v[40:41], v[40:41], v[28:29], v[44:45]\n\t"
      "v_pk_fma_f32 v[42:43], v[42:43], v[30:31], v[46:47]\n\t"
      "v_pk_fma_f32 v[56:57], v[52:53], v[48:49], v[56:57]\n\t"
      "v_pk_fma_f32 v[58:59], v[54:55], v[50:51], v[58:59]\n\t"
      "v_pk_fma_f32 v[60:61], v[40:41], v[48:49], v[60:61]\n\t"
      "v_pk_fma_f32 v[62:63], v[42:43], v[50:51], v[62:63]\n\t"
      "s_cbranch_scc1 Lstub2%=\n\t"
      "Lstep3%=:\n\t"
      "s_waitcnt vmcnt(4) lgkmcnt(2)\n\t"
      "v_mfma_f32_4x4x4_16b_bf16 v[40:43], v[0:1], v[90:91], 0\n\t"
      "v_exp_f32_e64 v32, v32 clamp\n\t"
      "v_mfma_f32_4x4x4_16b_bf16 v[44:47], v[0:1], v[94:95], 0\n\t"
      "v_exp_f32_e64 v33, v33 clamp\n\t"
      "v_mfma_f32_4x4x4_16b_bf16 v[24:27], v[0:1], v[64:65], v[6:9]\n\t"
      "v_exp_f32_e64 v34, v34 clamp\n\t"
      "v_mfma_f32_4x4x4_16b_bf16 v[28:31], v[0:1], v[68:69], v[6:9]\n\t"
      "v_exp_f32_e64 v35, v35 clamp\n\t"
      "s_sub_u32 s51, s51, 1\n\t"
      "v_exp_f32_e64 v36, v36 clamp\n\t"
      "s_cselect_b32 s42, 0, s42\n\t"
      "v_exp_f32_e64 v37, v37 clamp\n\t"
      "buffer_load_dwordx4 v[88:91], v[12:13], s[40:43], 0 idxen offen sc1\n\t"
      "v_exp_f32_e64 v38, v38 clamp\n\t"
      "buffer_load_dwordx4 v[92:95], v[14:15], s[40:43], 0 idxen offen sc1\n\t"
      "v_exp_f32_e64 v39, v39 clamp\n\t"
      "s_sub_u32 s50, s50, 1\n\t"
      "v_pk_add_f32 v[32:33], v[32:33], s[48:49]\n\t"
      "v_pk_add_f32 v[36:37], v[36:37], s[48:49]\n\t"
      "v_pk_add_f32 v[34:35], v[34:35], s[48:49]\n\t"
      "v_pk_add_f32 v[38:39], v[38:39], s[48:49]\n\t"
      "v_pk_mul_f32 v[48:49], v[32:33], v[36:37]\n\t"
      "v_pk_mul_f32 v[50:51], v[34:35], v[38:39]\n\t"
      "v_rcp_f32_e32 v48, v48\n\t"
      "ds_read_u16 v12, v16 offset:512\n\t"
      "v_rcp_f32_e32 v49, v49\n\t"
      "ds_read_u16 v14, v16 offset:544\n\t"
      "v_rcp_f32_e32 v50, v50\n\t"
      "s_waitcnt lgkmcnt(2)\n\t"
      "v_rcp_f32_e32 v51, v51\n\t"
      "v_pk_fma_f32 v[24:25], v[2:3], v[10:11], v[24:25] op_sel_hi:[1,0,1]\n\t"
      "v_pk_fma_f32 v[26:27], v[4:5], v[10:11], v[26:27] op_sel_hi:[1,0,1]\n\t"
      "v_pk_fma_f32 v[28:29], v[2:3], v[10:11], v[28:29] op_sel:[0,1,0] op_sel_hi:[1,1,1]\n\t"
      "v_pk_fma_f32 v[30:31], v[4:5], v[10:11], v[30:31] op_sel:[0,1,0] op_sel_hi:[1,1,1]\n\t"
      "ds_read_u16_d16_hi v10, v16 offset:322\n\t"
      "ds_read_u16_d16_hi v11, v16 offset:354\n\t"
      "v_pk_add_f32 v[52:53], v[32:33], v[36:37]\n\t"
      "v_pk_add_f32 v[54:55], v[34:35], v[38:39]\n\t"
      "v_pk_mul_f32 v[44:45], v[44:45], v[32:33]\n\t"
      "v_pk_mul_f32 v[46:47], v[46:47], v[34:35]\n\t"
      "v_pk_fma_f32 v[40:41], v[40:41], v[36:37], v[44:45]\n\t"
      "v_pk_fma_f32 v[42:43], v[42:43], v[38:39], v[46:47]\n\t"
      "v_add_u32 v16, 0x100, v16\n\t"
      "v_pk_fma_f32 v[56:57], v[52:53], v[48:49], v[56:57]\n\t"
      "v_pk_fma_f32 v[58:59], v[54:55], v[50:51], v[58:59]\n\t"
      "v_pk_fma_f32 v[60:61], v[40:41], v[48:49], v[60:61]\n\t"
      "v_pk_fma_f32 v[62:63], v[42:43], v[50:51], v[62:63]\n\t"
      "s_cbranch_scc0 Lstep0%=\n\t"
      "s_swappc_b64 s[54:55], s[52:53]\n\t"
      "s_branch Lstep0%=\n\t"
      "Lstub0%=:\n\t"
      "s_swappc_b64 s[54:55], s[52:53]\n\t"
      "s_branch Lstep1%=\n\t"
      "Lstub1%=:\n\t"
      "s_swappc_b64 s[54:55], s[52:53]\n\t"
      "s_branch Lstep2%=\n\t"
      "Lstub2%=:\n\t"
      "s_swappc_b64 s[54:55], s[52:53]\n\t"
      "s_branch Lstep3%=\n\t"
      "LA0%=:\n\t"
      "v_mfma_f32_4x4x4_16b_bf16 v[6:9], v[0:1], v[100:101], v[120:123]\n\t"
      "s_add_u32 s52, s60, LB0%=-Lpc%=\n\t"
      "s_addc_u32 s53, s61, 0\n\t"
      "s_mov_b32 s50, 0\n\t"
      "s_setpc_b64 s[54:55]\n\t"
      "LB0%=:\n\t"
      "v_mfma_f32_4x4x4_16b_bf16 v[52:55], v[0:1], v[98:99], 0\n\t"
      "s_setprio 1\n\t"
      "s_cmp_eq_u32 s59, 0\n\t"
      "s_cbranch_scc1 Lpr1%=\n\t"
      "s_setprio 2\n\t"
      "Lpr1%=:\n\t"
      "v_rcp_f32_e32 v48, v56\n\t"
      "v_rcp_f32_e32 v49, v57\n\t"
      "v_rcp_f32_e32 v50, v58\n\t"
      "v_rcp_f32_e32 v51, v59\n\t"
      "v_pk_mov_b32 v[56:57], s[62:63], s[62:63]\n\t"
      "v_pk_mov_b32 v[58:59], s[62:63], s[62:63]\n\t"
      "v_pk_fma_f32 v[52:53], v[60:61], v[48:49], v[52:53]\n\t"
      "v_pk_fma_f32 v[54:55], v[62:63], v[50:51], v[54:55]\n\t"
      "v_max_f32_e32 v52, 0, v52\n\t"
      "v_max_f32_e32 v53, 0, v53\n\t"
      "v_max_f32_e32 v54, 0, v54\n\t"
      "v_max_f32_e32 v55, 0, v55\n\t"
      "v_pk_mov_b32 v[60:61], 0, 0\n\t"
      "v_pk_mov_b32 v[62:63], 0, 0\n\t"
      "v_mfma_f32_4x4x4_16b_bf16 v[52:55], v[0:1], v[112:113], v[52:55]\n\t"
      "s_add_u32 s52, s60, LA1%=-Lpc%=\n\t"
      "s_addc_u32 s53, s61, 0\n\t"
      "s_cmp_eq_u32 s57, 0\n\t"
      "s_nop 1\n\t"
      "v_cvt_pk_bf16_f32 v22, v52, v53\n\t"
      "v_cvt_pk_bf16_f32 v23, v54, v55\n\t"
      "buffer_store_dwordx2 v[22:23], v17, s[44:47], 0 offen sc1\n\t"
      "s_cbranch_scc1 Lend%=\n\t"
      "s_sub_u32 s50, s57, 2\n\t"
      "s_cbranch_scc1 LA1%=\n\t"
      "s_setpc_b64 s[54:55]\n\t"
      "LA1%=:\n\t"
      "v_mfma_f32_4x4x4_16b_bf16 v[6:9], v[0:1], v[104:105], v[120:123]\n\t"
      "s_add_u32 s52, s60, LB1%=-Lpc%=\n\t"
      "s_addc_u32 s53, s61, 0\n\t"
      "s_mov_b32 s50, 0\n\t"
      "s_setpc_b64 s[54:55]\n\t"
      "LB1%=:\n\t"
      "v_mfma_f32_4x4x4_16b_bf16 v[52:55], v[0:1], v[102:103], 0\n\t"
      "s_setprio 0\n\t"
      "s_cmp_eq_u32 s59, 0\n\t"
      "s_cbranch_scc1 Lpr2%=\n\t"
      "s_setprio 1\n\t"
      "Lpr2%=:\n\t"
      "v_rcp_f32_e32 v48, v56\n\t"
      "v_rcp_f32_e32 v49, v57\n\t"
      "v_rcp_f32_e32 v50, v58\n\t"
      "v_rcp_f32_e32 v51, v59\n\t"
      "v_pk_mov_b32 v[56:57], s[62:63], s[62:63]\n\t"
      "v_pk_mov_b32 v[58:59], s[62:63], s[62:63]\n\t"
      "v_pk_fma_f32 v[52:53], v[60:61], v[48:49], v[52:53]\n\t"
      "v_pk_fma_f32 v[54:55], v[62:63], v[50:51], v[54:55]\n\t"
      "v_max_f32_e32 v52, 0, v52\n\t"
      "v_max_f32_e32 v53, 0, v53\n\t"
      "v_max_f32_e32 v54, 0, v54\n\t"
      "v_max_f32_e32 v55, 0, v55\n\t"
      "v_pk_mov_b32 v[60:61], 0, 0\n\t"
      "v_pk_mov_b32 v[62:63], 0, 0\n\t"
      "v_mfma_f32_4x4x4_16b_bf16 v[52:55], v[0:1], v[114:115], v[52:55]\n\t"
      "s_add_u32 s52, s60, LA2%=-Lpc%=\n\t"
      "s_addc_u32 s53, s61, 0\n\t"
      "s_cmp_eq_u32 s58, 0\n\t"
      "s_nop 1\n\t"
      "v_cvt_pk_bf16_f32 v22, v52, v53\n\t"
      "v_cvt_pk_bf16_f32 v23, v54, v55\n\t"
      "buffer_store_dwordx2 v[22:23], v18, s[44:47], 0 offen sc1\n\t"
      "s_cbranch_scc1 Lend%=\n\t"
      "s_sub_u32 s50, s58, 2\n\t"
      "s_cbranch_scc1 LA2%=\n\t"
      "s_setpc_b64 s[54:55]\n\t"
      "LA2%=:\n\t"
      "v_mfma_f32_4x4x4_16b_bf16 v[6:9], v[0:1], v[108:109], v[120:123]\n\t"
      "s_add_u32 s52, s60, LB2%=-Lpc%=\n\t"
      "s_addc_u32 s53, s61, 0\n\t"
      "s_mov_b32 s50, 0\n\t"
      "s_setpc_b64 s[54:55]\n\t"
      "LB2%=:\n\t"
      "v_mfma_f32_4x4x4_16b_bf16 v[52:55], v[0:1], v[106:107], 0\n\t"
      "s_setprio 0\n\t"
      "v_rcp_f32_e32 v48, v56\n\t"
      "v_rcp_f32_e32 v49, v57\n\t"
      "v_rcp_f32_e32 v50, v58\n\t"
      "v_rcp_f32_e32 v51, v59\n\t"
      "v_pk_mov_b32 v[56:57], s[62:63], s[62:63]\n\t"
      "v_pk_mov_b32 v[58:59], s[62:63], s[62:63]\n\t"
      "v_pk_fma_f32 v[52:53], v[60:61], v[48:49], v[52:53]\n\t"
      "v_pk_fma_f32 v[54:55], v[62:63], v[50:51], v[54:55]\n\t"
      "v_max_f32_e32 v52, 0, v52\n\t"
      "v_max_f32_e32 v53, 0, v53\n\t"
      "v_max_f32_e32 v54, 0, v54\n\t"
      "v_max_f32_e32 v55, 0, v55\n\t"
      "v_pk_mov_b32 v[60:61], 0, 0\n\t"
      "v_pk_mov_b32 v[62:63], 0, 0\n\t"
      "v_mfma_f32_4x4x4_16b_bf16 v[52:55], v[0:1], v[116:117], v[52:55]\n\t"
      "s_add_u32 s52, s60, LA3%=-Lpc%=\n\t"
      "s_addc_u32 s53, s61, 0\n\t"
      "s_cmp_eq_u32 s59, 0\n\t"
      "s_nop 1\n\t"
      "v_cvt_pk_bf16_f32 v22, v52, v53\n\t"
      "v_cvt_pk_bf16_f32 v23, v54, v55\n\t"
      "buffer_store_dwordx2 v[22:23], v19, s[44:47], 0 offen sc1\n\t"
      "s_cbranch_scc1 Lend%=\n\t"
      "s_sub_u32 s50, s59, 2\n\t"
      "s_cbranch_scc1 LA3%=\n\t"
      "s_setpc_b64 s[54:55]\n\t"
      "LA3%=:\n\t"
      "s_add_u32 s52, s60, LB3%=-Lpc%=\n\t"
      "s_addc_u32 s53, s61, 0\n\t"
      "s_mov_b32 s50, 0\n\t"
      "s_setpc_b64 s[54:55]\n\t"
      "LB3%=:\n\t"
      "v_mfma_f32_4x4x4_16b_bf16 v[52:55], v[0:1], v[110:111], 0\n\t"
      "v_rcp_f32_e32 v48, v56\n\t"
      "v_rcp_f32_e32 v49, v57\n\t"
      "v_rcp_f32_e32 v50, v58\n\t"
      "v_rcp_f32_e32 v51, v59\n\t"
      "v_pk_mov_b32 v[56:57], s[62:63], s[62:63]\n\t"
      "v_pk_mov_b32 v[58:59], s[62:63], s[62:63]\n\t"
      "v_pk_fma_f32 v[52:53], v[60:61], v[48:49], v[52:53]\n\t"
      "v_pk_fma_f32 v[54:55], v[62:63], v[50:51], v[54:55]\n\t"
      "v_max_f32_e32 v52, 0, v52\n\t"
      "v_max_f32_e32 v53, 0, v53\n\t"
      "v_max_f32_e32 v54, 0, v54\n\t"
      "v_max_f32_e32 v55, 0, v55\n\t"
      "v_pk_mov_b32 v[60:61], 0, 0\n\t"
      "v_pk_mov_b32 v[62:63], 0, 0\n\t"
      "v_mfma_f32_4x4x4_16b_bf16 v[52:55], v[0:1], v[118:119], v[52:55]\n\t"
      "s_nop 4\n\t"
      "v_cvt_pk_bf16_f32 v22, v52, v53\n\t"
      "v_cvt_pk_bf16_f32 v23, v54, v55\n\t"
      "buffer_store_dwordx2 v[22:23], v20, s[44:47], 0 offen sc1\n\t"
      "Lend%=:\n\t"
      "Lfused_last:\n\t"
      ASM_TAIL
      : "+{v16}"(lb), "+{s[40:43]}"(srd_tab), "+{s51}"(rem)
      : "{v[0:1]}"(ident), "{v[2:5]}"(cw), "{v13}"(coff), "{v17}"(off[0]), "{v18}"(off[1]), "{v19}"(off[2]),
        "{v20}"(off[3]), "{v[96:99]}"(au[0]), "{v[100:103]}"(au[1]), "{v[104:107]}"(au[2]), "{v[108:111]}"(au[3]),
        "{v[112:113]}"(xv[0]), "{v[114:115]}"(xv[1]), "{v[116:117]}"(xv[2]), "{v[118:119]}"(xv[3]),
        "{s[44:47]}"(srd_out), "{s[56:59]}"(np4)
      : "memory", "scc", "s48", "s49", "s50", "s52", "s53", "s54", "s55", "s60", "s61", "s62", "s63",
        "v6", "v7", "v8", "v9", "v10", "v11", "v12", "v14", "v15", "v22", "v23", "v24", "v25", "v26",
        "v27", "v28", "v29", "v30", "v31", "v32", "v33", "v34", "v35", "v36", "v37", "v38", "v39", "v40",
        "v41", "v42", "v43", "v44", "v45", "v46", "v47", "v48", "v49", "v50", "v51", "v52", "v53", "v54",
        "v55", "v56", "v57", "v58", "v59", "v60", "v61", "v62", "v63", "v64", "v65", "v66", "v67", "v68",
        "v69", "v70", "v71", "v72", "v73", "v74", "v75", "v76", "v77", "v78", "v79", "v80", "v81", "v82",
        "v83", "v84", "v85", "v86", "v87", "v88", "v89", "v90", "v91", "v92", "v93", "v94", "v95", "v120",
        "v121", "v122", "v123");
    STAMP(13);
    STAMP_FLUSH(0, 16);
  }
  if (b == 255 && wave == 0) {
    asm volatile("s_waitcnt vmcnt(0) lgkmcnt(0)" ::: "memory");
    const int l = __lane_id();
    for (int k = 0; k < 3; ++k) poll_tagged<1>(ws + WS_FLAG + (k * 64 + l) * 128, true, tlo, thi);
    if (l == 0) store_tagged(ws + WS_EPOCH, 0, tlo, thi);
  }
}

}

extern "C" void kernel_launch(void* const* d_in, const int* in_sizes, int n_in, void* d_out, int out_size, void* d_ws,
                              size_t ws_size, hipStream_t stream) {
  k_fused<<<256, 1024, 0, stream>>>((const uint16_t*)d_in[0], (const uint16_t*)d_in[1], (const int*)d_in[2],
                                    (const int*)d_in[3], (const uint16_t*)d_in[4], (const uint16_t*)d_in[5],
                                    (const uint16_t*)d_in[6], (const uint16_t*)d_in[7], (const uint16_t*)d_in[8],
                                    (char*)d_ws, (uint16_t*)d_out);
}
```
